```python
import torch

def _softmax(x, axis=-1):
    return torch.softmax(x, dim=axis)
def _gelu(x):
    return torch.nn.functional.gelu(x, approximate="tanh")
def _silu(x):
    return torch.nn.functional.silu(x)
def _sigmoid(x):
    return torch.sigmoid(x)
def _layernorm(x, axis=-1, eps=1e-5):
    m = x.mean(dim=axis, keepdim=True)
    v = x.var(dim=axis, keepdim=True, unbiased=False)
    return (x - m) / torch.sqrt(v + eps)
def _scatter_add(tgt, idx, src, axis=0):
    return tgt.clone().index_add_(axis, idx, src)
def _segment_sum(src, seg, n):
    out = torch.zeros((n,) + src.shape[1:], dtype=src.dtype)
    return out.index_add_(0, seg, src)
def _pad(x, pw):
    flat = tuple(v for pair in reversed(pw) for v in pair)
    return torch.nn.functional.pad(x, flat)

kernel_name = 'parallel_sum__linear_kernel_hedgehog__dense_activation_squared_relu__prompt'

def setup_inputs():

    _w = (lambda a: (lambda t: t.to(torch.bfloat16) if t.is_floating_point() else t)(torch.as_tensor(a)))
    def _A_setup():
        B, S, D, H = 1, 2048, 512, 32
        Dh = D // H
        return {
            "x": _w((lambda *s: torch.randn(*s, dtype=torch.bfloat16) * 0.05)(B, S, D)),
            "wqkv": _w((lambda *s: torch.randn(*s, dtype=torch.bfloat16) * 0.8)(D, 3 * D)),
            "w_phi": _w((lambda *s: torch.randn(*s, dtype=torch.bfloat16) * 0.8)(H, Dh, Dh) * (Dh ** -0.5)),
            "wo": _w((lambda *s: torch.randn(*s, dtype=torch.bfloat16) * 0.2)(D, D)),
        }
    _A_in = _A_setup()

    def _B_setup():
        B, S, D, F = 1, 2048, 512, 1792
        return {
            "x": _w((lambda *s: torch.randn(*s, dtype=torch.bfloat16) * 0.05)(B, S, D)),
            "w1": _w((lambda *s: torch.randn(*s, dtype=torch.bfloat16) * 0.05)(D, F)),
            "b1": _w((lambda *s: torch.randn(*s, dtype=torch.bfloat16) * 0.4)(F)),
            "w2": _w((lambda *s: torch.randn(*s, dtype=torch.bfloat16) * 0.05)(F, D)),
            "b2": _w((lambda *s: torch.randn(*s, dtype=torch.bfloat16) * 3.2)(D)),
        }
    _B_in = _B_setup()

    return {
        "A_x": _A_in["x"],
        "A_wqkv": _A_in["wqkv"],
        "A_w_phi": _A_in["w_phi"],
        "A_wo": _A_in["wo"],
        "B_x": _B_in["x"],
        "B_w1": _B_in["w1"],
        "B_b1": _B_in["b1"],
        "B_w2": _B_in["w2"],
        "B_b2": _B_in["b2"],
    }

def reference(A_x, A_wqkv, A_w_phi, A_wo, B_x, B_w1, B_b1, B_w2, B_b2):
    def _A_ref(x, wqkv, w_phi, wo):
        B, S, D = x.shape
        H = 32; Dh = D // H
        qkv = (lambda x, *d: x.permute(*d))((x @ wqkv).reshape(B, S, 3, H, Dh), 2, 0, 3, 1, 4)
        q, k, v = qkv[0], qkv[1], qkv[2]
        pq = torch.einsum("bhsd,hdf->bhsf", q, w_phi)
        pk = torch.einsum("bhsd,hdf->bhsf", k, w_phi)
        phi_q = torch.concatenate([_softmax(pq, axis=-1), _softmax(-pq, axis=-1)], axis=-1)
        phi_k = torch.concatenate([_softmax(pk, axis=-1), _softmax(-pk, axis=-1)], axis=-1)
        kv = torch.einsum("bhsf,bhsd->bhfd", phi_k, v)
        z  = torch.einsum("bhsf->bhf", phi_k)
        num = torch.einsum("bhqf,bhfd->bhqd", phi_q, kv)
        den = torch.einsum("bhqf,bhf->bhq", phi_q, z)[..., None] + 1e-6
        o = num / den
        return (lambda x, *d: x.permute(*d))(o, 0, 2, 1, 3).reshape(B, S, D) @ wo

    def _B_ref(x, w1, b1, w2, b2):
        h = torch.einsum("bsd,df->bsf", x, w1) + b1
        zero = torch.zeros_like(h)
        r = torch.where(h > zero, h, zero)
        h = r * r
        out = torch.einsum("bsf,fd->bsd", h, w2) + b2
        return out

    x = A_x
    xn = _layernorm(x, axis=-1)
    return x + _A_ref(xn, A_wqkv, A_w_phi, A_wo) + _B_ref(xn, B_w1, B_b1, B_w2, B_b2)
```

```cpp
#include <hip/hip_runtime.h>
#include <hip/hip_bf16.h>
#include <stdint.h>

#define LAS __attribute__((address_space(3)))
#define GAS __attribute__((address_space(1)))
typedef __bf16 bf16x8 __attribute__((ext_vector_type(8)));
typedef __bf16 bf16x2 __attribute__((ext_vector_type(2)));
typedef short s16x4 __attribute__((ext_vector_type(4)));
typedef short s16x8 __attribute__((ext_vector_type(8)));
typedef float f32x16 __attribute__((ext_vector_type(16)));
typedef float f32x4 __attribute__((ext_vector_type(4)));
typedef float f32x2 __attribute__((ext_vector_type(2)));
typedef unsigned u32x4 __attribute__((ext_vector_type(4)));
typedef unsigned u32x2 __attribute__((ext_vector_type(2)));
typedef unsigned short u16;

constexpr int S_ = 2048, D_ = 512, F_ = 1792, H_ = 32;
constexpr size_t WS_HF = 0;
constexpr size_t WS_PQ = WS_HF + (size_t)64 * 112 * 1024;
constexpr size_t WS_KVP = WS_PQ + (size_t)64 * 32 * 2048;
constexpr size_t WS_KVZ = WS_KVP + (size_t)16 * 32 * 2176;
constexpr size_t WS_END = WS_KVZ + 32 * 4 * 17 * 16;

__device__ __forceinline__ void glds16(const void* sbase, unsigned voff, unsigned lds_dst) {
  asm volatile("s_mov_b32 m0, %2\n\ts_nop 0\n\tglobal_load_lds_dwordx4 %0, %1" ::"v"(voff), "s"(sbase), "s"(lds_dst) : "memory");
}
#define WAIT_V(n) asm volatile("s_waitcnt vmcnt(%0)" ::"i"(n) : "memory")
#define WAIT_L0() asm volatile("s_waitcnt lgkmcnt(0)" ::: "memory")
#define BAR() do { asm volatile("" ::: "memory"); __builtin_amdgcn_s_barrier(); asm volatile("" ::: "memory"); } while (0)

__device__ __forceinline__ bf16x8 tr_frag(LAS const char* p0, LAS const char* p1) {
  s16x4 a = __builtin_amdgcn_ds_read_tr16_b64_v4i16((LAS s16x4*)p0);
  s16x4 b = __builtin_amdgcn_ds_read_tr16_b64_v4i16((LAS s16x4*)p1);
  s16x8 c = __builtin_shufflevector(a, b, 0, 1, 2, 3, 4, 5, 6, 7);
  return __builtin_bit_cast(bf16x8, c);
}
__device__ __forceinline__ f32x16 mfma32(bf16x8 a, bf16x8 b, f32x16 c) { return __builtin_amdgcn_mfma_f32_32x32x16_bf16(a, b, c, 0, 0, 0); }
__device__ __forceinline__ unsigned pk2(float a, float b) {
  bf16x2 v = {(__bf16)a, (__bf16)b};
  return __builtin_bit_cast(unsigned, v);
}
__device__ __forceinline__ bf16x8 pack8(const f32x16& v, int base, float s) {
  u32x4 r;
  r[0] = pk2(v[base + 0] * s, v[base + 1] * s); r[1] = pk2(v[base + 2] * s, v[base + 3] * s);
  r[2] = pk2(v[base + 4] * s, v[base + 5] * s); r[3] = pk2(v[base + 6] * s, v[base + 7] * s);
  return __builtin_bit_cast(bf16x8, r);
}
__device__ __forceinline__ float dot2(unsigned a, unsigned b, float c) {
  return __builtin_amdgcn_fdot2_f32_bf16(__builtin_bit_cast(bf16x2, a), __builtin_bit_cast(bf16x2, b), c, false);
}
__device__ __forceinline__ float xor32_add(float v) { return v + __shfl_xor(v, 32); }

__device__ __forceinline__ void softmax16(const f32x16& p, u32x4& f0, u32x4& f1) {
  float mx = fmaxf(fmaxf(p[0], p[1]), fmaxf(p[2], p[3]));
#pragma unroll
  for (int i = 4; i < 16; i += 2) mx = fmaxf(mx, fmaxf(p[i], p[i + 1]));
  const float L2E = 1.4426950408889634f;
  const float t = -mx * L2E;
  float e[16]; float s = 0.f;
#pragma unroll
  for (int i = 0; i < 16; ++i) { e[i] = __builtin_amdgcn_exp2f(__builtin_fmaf(p[i], L2E, t)); s += e[i]; }
  const float r = __builtin_amdgcn_rcpf(s);
  f0[0] = pk2(e[0] * r, e[1] * r); f0[1] = pk2(e[2] * r, e[3] * r); f0[2] = pk2(e[4] * r, e[5] * r); f0[3] = pk2(e[6] * r, e[7] * r);
  f1[0] = pk2(e[8] * r, e[9] * r); f1[1] = pk2(e[10] * r, e[11] * r); f1[2] = pk2(e[12] * r, e[13] * r); f1[3] = pk2(e[14] * r, e[15] * r);
}

constexpr int P1_SLOT = 45056;
constexpr int P1_XOFF = 28672;
constexpr int P1_MISC = 3 * P1_SLOT;
constexpr int P1_WPHI = P1_MISC + 7168;
constexpr int P1_ONES = P1_WPHI + 2048;
constexpr int P1_LDS = P1_ONES + 64;

template <bool TA>
__device__ __forceinline__ void phase1_body(LAS char* lds, unsigned lds0, int panel, int role, const u16* __restrict__ x, const u16* __restrict__ wqkv,
                                            const u16* __restrict__ wphi, const u16* __restrict__ w1, const u16* __restrict__ b1, char* __restrict__ ws) {
  constexpr int NT = TA ? 6 : 7;
  constexpr int ROWB = TA ? 384 : 448;
  constexpr int NCH = ROWB / 16;
  constexpr int NP = NT;
  constexpr int WROW = TA ? 3072 : 3584;
  const int tid = threadIdx.x, l = tid & 63, w = __builtin_amdgcn_readfirstlane(tid >> 6);
  const int t = l & 31, hh = l >> 5;
  unsigned woff[NP];
#pragma unroll
  for (int i = 0; i < NP; ++i) {
    const int n = 64 * i + l, row = n / NCH, c = n % NCH;
    if (TA) { const int cl = c ^ (((row >> 1) & 1) << 2); woff[i] = row * WROW + (cl >> 3) * 1024 + role * 128 + (cl & 7) * 16; }
    else woff[i] = row * WROW + role * 448 + c * 16;
  }
  unsigned xoff[4];
#pragma unroll
  for (int i = 0; i < 4; ++i) { const int row = 8 * i + (l >> 3), c = (l & 7) ^ ((row >> 1) & 7); xoff[i] = row * 1024 + c * 16; }
  const char* wbase = (const char*)(TA ? wqkv : w1) + (size_t)(16 * w) * WROW;
  const char* xbase = (const char*)x + (size_t)(panel * 128 + 32 * w) * 1024;
  auto issue = [&](int kc) {
    const unsigned slot = lds0 + (kc % 3) * P1_SLOT;
#pragma unroll
    for (int i = 0; i < NP; ++i) glds16(wbase + (size_t)kc * 64 * WROW, woff[i], slot + w * 16 * ROWB + i * 1024);
#pragma unroll
    for (int i = 0; i < 4; ++i) glds16(xbase + kc * 128, xoff[i], slot + P1_XOFF + w * 4096 + i * 1024);
  };
  if (TA) {
    if (w == 0) { glds16((const char*)wphi + role * 2048, l * 16, lds0 + P1_WPHI); glds16((const char*)wphi + role * 2048 + 1024, l * 16, lds0 + P1_WPHI + 1024); }
    if (tid < 4) *(LAS unsigned*)(lds + P1_ONES + tid * 4) = 0x3F803F80u;
  }
  issue(0); issue(1);
  const int blk = (l >> 4) & 1, q = (l & 15) >> 2, p = l & 3;
  const int wrow = (8 * hh + q) * ROWB + 32 * blk + 8 * p;
  const int sw = TA ? ((q >> 1) & 1) * 64 : 0;
  const int wrd_e = wrow + sw, wrd_o = wrow - sw;
  int xrd[4];
#pragma unroll
  for (int ks = 0; ks < 4; ++ks) xrd[ks] = P1_XOFF + w * 4096 + t * 128 + (((2 * ks + hh) ^ ((t >> 1) & 7)) << 4);
  f32x16 acc[NT];
#pragma unroll
  for (int i = 0; i < NT; ++i) acc[i] = (f32x16){};
  float csp[NT];
#pragma unroll
  for (int i = 0; i < NT; ++i) csp[i] = 0.f;
  float sum = 0.f, sq = 0.f;
  const unsigned ONE2 = 0x3F803F80u;
#pragma unroll 1
  for (int kc = 0; kc < 8; ++kc) {
    if (kc < 7) WAIT_V(NP + 4); else WAIT_V(0);
    WAIT_L0();
    BAR();
    if (kc + 2 < 8) issue(kc + 2);
    LAS const char* sl = lds + (kc % 3) * P1_SLOT;
#pragma unroll
    for (int ks = 0; ks < 4; ++ks) {
      const bf16x8 xf = *(LAS const bf16x8*)(sl + xrd[ks]);
      const u32x4 xu = __builtin_bit_cast(u32x4, xf);
#pragma unroll
      for (int i = 0; i < 4; ++i) { sum = dot2(xu[i], ONE2, sum); sq = dot2(xu[i], xu[i], sq); }
#pragma unroll
      for (int i = 0; i < NT; ++i) {
        LAS const char* a = sl + ks * 16 * ROWB + ((i & 1) ? wrd_o : wrd_e) + 64 * i;
        const bf16x8 wf = tr_frag(a, a + 4 * ROWB);
        acc[i] = mfma32(wf, xf, acc[i]);
        if (ks == w) {
          const u32x4 wu = __builtin_bit_cast(u32x4, wf);
#pragma unroll
          for (int j = 0; j < 4; ++j) csp[i] = dot2(wu[j], ONE2, csp[i]);
        }
      }
    }
  }
  sum = xor32_add(sum); sq = xor32_add(sq);
  const float mean = sum * (1.f / 512.f);
  const float var = fmaxf(sq * (1.f / 512.f) - mean * mean, 0.f);
  const float rstd = 1.f / sqrtf(var + 1e-5f);
#pragma unroll
  for (int i = 0; i < NT; ++i) *(LAS float*)(lds + P1_MISC + (i * 64 + l) * 16 + w * 4) = csp[i];
  WAIT_L0();
  BAR();
#pragma unroll
  for (int i = 0; i < NT; ++i) {
    const f32x4 c4 = *(LAS const f32x4*)(lds + P1_MISC + (i * 64 + l) * 16);
    float cs = (c4[0] + c4[1]) + (c4[2] + c4[3]);
    if (TA) acc[i] = __builtin_amdgcn_mfma_f32_32x32x2f32(cs, -mean, acc[i], 0, 0, 0);
    else {
      cs = xor32_add(cs);
      const float bb = (float)__builtin_bit_cast(__bf16, b1[role * 224 + 32 * i + t]);
      acc[i] = __builtin_amdgcn_mfma_f32_32x32x2f32(hh ? bb : cs, hh ? (var + 1e-5f) * rstd : -mean, acc[i], 0, 0, 0);
    }
  }
  const int tok32 = panel * 4 + w;
  if (!TA) {
    char* hf = ws + WS_HF + ((size_t)tok32 * 112 + role * 14) * 1024 + l * 16;
#pragma unroll
    for (int i = 0; i < NT; ++i) {
      u32x4 o[2];
#pragma unroll
      for (int j = 0; j < 8; ++j) {
        const float a = fmaxf(acc[i][2 * j] * rstd, 0.f), b = fmaxf(acc[i][2 * j + 1] * rstd, 0.f);
        o[j >> 2][j & 3] = pk2(a * a, b * b);
      }
      *(u32x4*)(hf + (2 * i) * 1024) = o[0];
      *(u32x4*)(hf + (2 * i + 1) * 1024) = o[1];
    }
  } else {
    const unsigned sgn = ((t >> 2) & 1) ? 0x80008000u : 0u;
    bf16x8 aphi[4];
#pragma unroll
    for (int h = 0; h < 4; ++h) {
      LAS const char* a = lds + P1_WPHI + h * 512 + (4 * hh + q) * 32 + (8 * blk + 4 * (p >> 1)) * 2;
      u32x4 u = __builtin_bit_cast(u32x4, tr_frag(a, a + 8 * 32));
      u[0] ^= sgn; u[1] ^= sgn; u[2] ^= sgn; u[3] ^= sgn;
      aphi[h] = __builtin_bit_cast(bf16x8, u);
    }
    char* pq = ws + WS_PQ + ((size_t)tok32 * 32 + role * 4) * 2048 + l * 16;
    LAS char* PK = lds;
    LAS char* VI = lds + 32768;
    const int tok = 32 * w + t, xs = (tok >> 1) & 3;
#pragma unroll
    for (int h = 0; h < 4; ++h) {
      const bf16x8 qf = pack8(acc[h >> 1], 8 * (h & 1), rstd);
      const f32x16 pp = mfma32(aphi[h], qf, (f32x16){});
      u32x4 f0, f1; softmax16(pp, f0, f1);
      *(u32x4*)(pq + h * 2048) = f0;
      *(u32x4*)(pq + h * 2048 + 1024) = f1;
    }
#pragma unroll
    for (int h = 0; h < 4; ++h) {
      const bf16x8 kf = pack8(acc[2 + (h >> 1)], 8 * (h & 1), rstd);
      const f32x16 pp = mfma32(aphi[h], kf, (f32x16){});
      u32x4 f0, f1; softmax16(pp, f0, f1);
      LAS char* r = PK + h * 8192 + tok * 64;
      *(LAS u32x4*)(r + (((2 * hh) ^ xs) << 4)) = f0;
      *(LAS u32x4*)(r + (((2 * hh + 1) ^ xs) << 4)) = f1;
    }
#pragma unroll
    for (int pr = 0; pr < 2; ++pr) {
      LAS char* r = VI + pr * 8192 + tok * 64 + 8 * hh;
#pragma unroll
      for (int g = 0; g < 4; ++g) {
        u32x2 o;
        o[0] = pk2(acc[4 + pr][4 * g] * rstd, acc[4 + pr][4 * g + 1] * rstd);
        o[1] = pk2(acc[4 + pr][4 * g + 2] * rstd, acc[4 + pr][4 * g + 3] * rstd);
        *(LAS u32x2*)(r + ((g ^ xs) << 4)) = o;
      }
    }
    WAIT_L0();
    BAR();
    f32x16 kv = (f32x16){};
#pragma unroll
    for (int u = 0; u < 8; ++u) {
      const int r0 = 16 * u + 4 * hh + q, r1 = r0 + 8;
      const int ca = 2 * blk + (p >> 1);
      LAS const char* a0 = PK + w * 8192 + r0 * 64 + ((ca ^ ((r0 >> 1) & 3)) << 4) + 8 * (p & 1);
      LAS const char* a1 = PK + w * 8192 + r1 * 64 + ((ca ^ ((r1 >> 1) & 3)) << 4) + 8 * (p & 1);
      const int cb = 2 * (w & 1) + (p >> 1);
      LAS const char* b0 = blk ? (lds + P1_ONES) : (VI + (w >> 1) * 8192 + r0 * 64 + ((cb ^ ((r0 >> 1) & 3)) << 4) + 8 * (p & 1));
      LAS const char* b1p = blk ? (lds + P1_ONES) : (VI + (w >> 1) * 8192 + r1 * 64 + ((cb ^ ((r1 >> 1) & 3)) << 4) + 8 * (p & 1));
      kv = mfma32(tr_frag(a0, a1), tr_frag(b0, b1p), kv);
    }
    if (t <= 16) {
      float* kp = (float*)(ws + WS_KVP + ((size_t)panel * 32 + role * 4 + w) * 2176) + t * 32 + 4 * hh;
#pragma unroll
      for (int g = 0; g < 4; ++g) *(f32x4*)(kp + 8 * g) = (f32x4){kv[4 * g], kv[4 * g + 1], kv[4 * g + 2], kv[4 * g + 3]};
    }
  }
}

__global__ __launch_bounds__(256, 1) void k_phase1(const u16* __restrict__ x, const u16* __restrict__ wqkv, const u16* __restrict__ wphi,
                                                   const u16* __restrict__ w1, const u16* __restrict__ b1, char* __restrict__ ws) {
  extern __shared__ __attribute__((aligned(16))) char smem[];
  LAS char* lds = (LAS char*)smem;
  const unsigned lds0 = (unsigned)(size_t)lds;
  const int b = blockIdx.x, xcd = b & 7, idx = b >> 3;
  const int panel = (xcd & 1) * 8 + (idx & 7), cg = xcd >> 1, sel = idx >> 3;
  const int role = cg + 4 * (sel & 1);
  if (sel < 2) phase1_body<true>(lds, lds0, panel, role, x, wqkv, wphi, w1, b1, ws);
  else phase1_body<false>(lds, lds0, panel, role, x, wqkv, wphi, w1, b1, ws);
}

__global__ __launch_bounds__(128) void k_kvred(char* __restrict__ ws) {
  const int h = blockIdx.x, i = threadIdx.x;
  if (i >= 68) return;
  const int c = i >> 2, fo = i & 3;
  const float* src = (const float*)(ws + WS_KVP + (size_t)h * 2176) + c * 32 + fo * 8;
  f32x4 a = {0.f, 0.f, 0.f, 0.f}, bq = {0.f, 0.f, 0.f, 0.f};
#pragma unroll
  for (int r = 0; r < 16; ++r) { a += *(const f32x4*)(src + (size_t)r * 32 * 544); bq += *(const f32x4*)(src + (size_t)r * 32 * 544 + 4); }
  u32x4 o; o[0] = pk2(a[0], a[1]); o[1] = pk2(a[2], a[3]); o[2] = pk2(bq[0], bq[1]); o[3] = pk2(bq[2], bq[3]);
  *(u32x4*)(ws + WS_KVZ + (((size_t)h * 4 + fo) * 17 + c) * 16) = o;
}

constexpr int P2_NS = 9;
constexpr int P2_WAVE = P2_NS * 4096;
constexpr int P2_LDS = 4 * P2_WAVE;

__global__ __launch_bounds__(256, 1) void k_phase2(const u16* __restrict__ x, const u16* __restrict__ wo, const u16* __restrict__ w2, const u16* __restrict__ b2,
                                                   const char* __restrict__ ws, u16* __restrict__ out) {
  extern __shared__ __attribute__((aligned(16))) char smem[];
  LAS char* lds = (LAS char*)smem;
  const unsigned lds0 = (unsigned)(size_t)lds;
  const int b = blockIdx.x, xcd = b & 7, idx = b >> 3;
  const int tt = 4 * xcd + (idx >> 3), ct = idx & 7;
  const int tid = threadIdx.x, l = tid & 63, w = __builtin_amdgcn_readfirstlane(tid >> 6);
  const int t = l & 31, hh = l >> 5;
  const int blk = (l >> 4) & 1, q = (l & 15) >> 2, p = l & 3;
  const unsigned wsrc = (l >> 3) * 1024 + (((l & 7) ^ ((((l >> 3) >> 1) & 1) << 2)) << 4);
  const unsigned lin = l * 16;
  const char* w2b = (const char*)w2 + (size_t)(448 * w) * 1024 + ct * 128;
  const char* wob = (const char*)wo + (size_t)(128 * w) * 1024 + ct * 128;
  const char* hfb = ws + WS_HF + ((size_t)(2 * tt) * 112 + 28 * w) * 1024;
  const char* pqb = ws + WS_PQ + ((size_t)(2 * tt) * 32 + 8 * w) * 2048;
  const unsigned ring = lds0 + w * P2_WAVE;
  const unsigned kvsrc0 = (t < 16 ? t : 16) * 16 + hh * 2 * 272;
  auto issue = [&](int n) {
    const unsigned dst = ring + (n % P2_NS) * 4096;
    if (n < 28) {
      glds16(w2b + (size_t)n * 16 * 1024, wsrc, dst);
      glds16(w2b + (size_t)n * 16 * 1024 + 8192, wsrc, dst + 1024);
      glds16(hfb + n * 1024, lin, dst + 2048);
      glds16(hfb + (112 + n) * 1024, lin, dst + 3072);
    } else if (n < 44) {
      const int hd = (n - 28) >> 1;
      if (((n - 28) & 1) == 0) {
        glds16(wob + (size_t)hd * 16 * 1024, wsrc, dst);
        glds16(wob + (size_t)hd * 16 * 1024 + 8192, wsrc, dst + 1024);
        glds16(ws + WS_KVZ + (size_t)(8 * w + hd) * 1088, kvsrc0, dst + 2048);
        glds16(ws + WS_KVZ + (size_t)(8 * w + hd) * 1088 + 272, kvsrc0, dst + 3072);
      } else {
        glds16(pqb + hd * 2048, lin, dst);
        glds16(pqb + hd * 2048 + 1024, lin, dst + 1024);
        glds16(pqb + (32 + hd) * 2048, lin, dst + 2048);
        glds16(pqb + (32 + hd) * 2048 + 1024, lin, dst + 3072);
      }
    }
  };
  const int arow = (4 * hh + q) * 128 + 8 * (p & 1);
  const int sw = (q >> 1) & 1;
  const int ard0 = w * P2_WAVE + arow + (((2 * blk + (p >> 1)) ^ (4 * sw)) << 4);
  const int ard1 = w * P2_WAVE + arow + (((4 + 2 * blk + (p >> 1)) ^ (4 * sw)) << 4);
  const int lrd = w * P2_WAVE + l * 16;
  f32x16 acc[2][2];
#pragma unroll
  for (int i = 0; i < 2; ++i)
#pragma unroll
    for (int j = 0; j < 2; ++j) acc[i][j] = (f32x16){};
#pragma unroll
  for (int n = 0; n < P2_NS - 1; ++n) issue(n);
#pragma unroll
  for (int n = 0; n < 28; ++n) {
    WAIT_L0();
    issue(n + P2_NS - 1);
    WAIT_V(4 * (P2_NS - 1));
    const int so = (n % P2_NS) * 4096;
    const bf16x8 a0 = tr_frag(lds + ard0 + so, lds + ard0 + so + 1024);
    const bf16x8 a1 = tr_frag(lds + ard1 + so, lds + ard1 + so + 1024);
    const bf16x8 b0 = *(LAS const bf16x8*)(lds + lrd + so + 2048);
    const bf16x8 b1 = *(LAS const bf16x8*)(lds + lrd + so + 3072);
    acc[0][0] = mfma32(a0, b0, acc[0][0]);
    acc[0][1] = mfma32(a0, b1, acc[0][1]);
    acc[1][0] = mfma32(a1, b0, acc[1][0]);
    acc[1][1] = mfma32(a1, b1, acc[1][1]);
  }
#pragma unroll
  for (int hd = 0; hd < 8; ++hd) {
    const int n = 28 + 2 * hd;
    WAIT_L0();
    issue(n + P2_NS - 1);
    {
      const int issued_last = (n + P2_NS - 1 < 44) ? (n + P2_NS - 1) : 43;
      const int inflight_ok = issued_last - (n + 1);
      switch (inflight_ok) {
        case 7: WAIT_V(28); break; case 6: WAIT_V(24); break; case 5: WAIT_V(20); break; case 4: WAIT_V(16); break;
        case 3: WAIT_V(12); break; case 2: WAIT_V(8); break; case 1: WAIT_V(4); break; default: WAIT_V(0); break;
      }
    }
    const int so = (n % P2_NS) * 4096, sp = ((n + 1) % P2_NS) * 4096;
    const bf16x8 a0 = tr_frag(lds + ard0 + so, lds + ard0 + so + 1024);
    const bf16x8 a1 = tr_frag(lds + ard1 + so, lds + ard1 + so + 1024);
    const bf16x8 kz0 = *(LAS const bf16x8*)(lds + lrd + so + 2048);
    const bf16x8 kz1 = *(LAS const bf16x8*)(lds + lrd + so + 3072);
#pragma unroll
    for (int j = 0; j < 2; ++j) {
      const bf16x8 p0 = *(LAS const bf16x8*)(lds + lrd + sp + j * 2048);
      const bf16x8 p1 = *(LAS const bf16x8*)(lds + lrd + sp + j * 2048 + 1024);
      f32x16 nm = mfma32(kz0, p0, (f32x16){});
      nm = mfma32(kz1, p1, nm);
      const float rd = __builtin_amdgcn_rcpf(nm[8] + 1e-6f);
      const bf16x8 of = pack8(nm, 0, rd);
      acc[0][j] = mfma32(a0, of, acc[0][j]);
      acc[1][j] = mfma32(a1, of, acc[1][j]);
    }
    WAIT_L0();
    issue(n + P2_NS);
  }
  WAIT_V(0);
  WAIT_L0();
  BAR();
#pragma unroll
  for (int i = 0; i < 2; ++i)
#pragma unroll
    for (int j = 0; j < 2; ++j)
#pragma unroll
      for (int g = 0; g < 4; ++g)
        *(LAS f32x4*)(lds + w * 17408 + (32 * j + t) * 272 + (32 * i + 8 * g + 4 * hh) * 4) =
            (f32x4){acc[i][j][4 * g], acc[i][j][4 * g + 1], acc[i][j][4 * g + 2], acc[i][j][4 * g + 3]};
  WAIT_L0();
  BAR();
  {
    const int tok = tid >> 2, n0 = (tid & 3) * 16;
    const size_t go = (size_t)(tt * 64 + tok) * 512 + ct * 64 + n0;
    const u32x4 xa = *(const u32x4*)(x + go), xb = *(const u32x4*)(x + go + 8);
    const u32x4 ba = *(const u32x4*)(b2 + ct * 64 + n0), bb = *(const u32x4*)(b2 + ct * 64 + n0 + 8);
    f32x4 s[4];
#pragma unroll
    for (int c = 0; c < 4; ++c) {
      s[c] = *(LAS const f32x4*)(lds + tok * 272 + (n0 + 4 * c) * 4);
#pragma unroll
      for (int ww = 1; ww < 4; ++ww) s[c] += *(LAS const f32x4*)(lds + ww * 17408 + tok * 272 + (n0 + 4 * c) * 4);
    }
    auto lo = [](unsigned u) { return __builtin_bit_cast(float, u << 16); };
    auto hi = [](unsigned u) { return __builtin_bit_cast(float, u & 0xffff0000u); };
    u32x4 oa, ob;
#pragma unroll
    for (int c = 0; c < 4; ++c) {
      const unsigned x0 = c < 2 ? xa[2 * c] : xb[2 * c - 4], x1 = c < 2 ? xa[2 * c + 1] : xb[2 * c - 3];
      const unsigned b0 = c < 2 ? ba[2 * c] : bb[2 * c - 4], b1 = c < 2 ? ba[2 * c + 1] : bb[2 * c - 3];
      const unsigned r0 = pk2(s[c][0] + lo(x0) + lo(b0), s[c][1] + hi(x0) + hi(b0));
      const unsigned r1 = pk2(s[c][2] + lo(x1) + lo(b1), s[c][3] + hi(x1) + hi(b1));
      if (c < 2) { oa[2 * c] = r0; oa[2 * c + 1] = r1; } else { ob[2 * c - 4] = r0; ob[2 * c - 3] = r1; }
    }
    *(u32x4*)(out + go) = oa;
    *(u32x4*)(out + go + 8) = ob;
  }
}

extern "C" void kernel_launch(void* const* d_in, const int* in_sizes, int n_in, void* d_out, int out_size, void* d_ws, size_t ws_size, hipStream_t stream) {
  const u16* x = (const u16*)d_in[0];
  const u16* wqkv = (const u16*)d_in[1];
  const u16* wphi = (const u16*)d_in[2];
  const u16* wo = (const u16*)d_in[3];
  const u16* w1 = (const u16*)d_in[5];
  const u16* b1 = (const u16*)d_in[6];
  const u16* w2 = (const u16*)d_in[7];
  const u16* b2 = (const u16*)d_in[8];
  char* ws = (char*)d_ws;
  hipFuncSetAttribute((const void*)k_phase1, hipFuncAttributeMaxDynamicSharedMemorySize, P1_LDS);
  hipFuncSetAttribute((const void*)k_phase2, hipFuncAttributeMaxDynamicSharedMemorySize, P2_LDS);
  k_phase1<<<256, 256, P1_LDS, stream>>>(x, wqkv, wphi, w1, b1, ws);
  k_kvred<<<32, 128, 0, stream>>>(ws);
  k_phase2<<<256, 256, P2_LDS, stream>>>(x, wo, w2, b2, ws, (u16*)d_out);
}
```

```cpp
#include <hip/hip_runtime.h>
#include <hip/hip_bf16.h>
#include <stdint.h>

#define LAS __attribute__((address_space(3)))
#define GAS __attribute__((address_space(1)))
typedef __bf16 bf16x8 __attribute__((ext_vector_type(8)));
typedef __bf16 bf16x2 __attribute__((ext_vector_type(2)));
typedef short s16x4 __attribute__((ext_vector_type(4)));
typedef short s16x8 __attribute__((ext_vector_type(8)));
typedef float f32x16 __attribute__((ext_vector_type(16)));
typedef float f32x4 __attribute__((ext_vector_type(4)));
typedef float f32x2 __attribute__((ext_vector_type(2)));
typedef unsigned u32x4 __attribute__((ext_vector_type(4)));
typedef unsigned u32x2 __attribute__((ext_vector_type(2)));
typedef unsigned short u16;
typedef GAS unsigned gu32;
typedef GAS unsigned long long gu64;
#define RLX_AGENT __ATOMIC_RELAXED, __HIP_MEMORY_SCOPE_AGENT

constexpr size_t WS_HF = 0;
constexpr size_t WS_PQ = WS_HF + (size_t)64 * 112 * 1024;
constexpr size_t WS_KVP = WS_PQ + (size_t)64 * 32 * 2048;
constexpr size_t WS_KVZ = WS_KVP + (size_t)16 * 32 * 2176;
constexpr size_t WS_FLG = WS_KVZ + 32 * 4 * 17 * 16 + 1024 - (32 * 4 * 17 * 16) % 1024;
constexpr size_t WS_WT = WS_FLG + 1024;
constexpr size_t WS_END = WS_WT + (size_t)144 * 16 * 1024;

__device__ __forceinline__ void glds16(const void* sbase, unsigned voff, unsigned lds_dst) {
  asm volatile("s_mov_b32 m0, %2\n\ts_nop 0\n\tglobal_load_lds_dwordx4 %0, %1" ::"v"(voff), "s"(sbase), "s"(lds_dst) : "memory");
}
__device__ __forceinline__ void glds16_lo(const void* sbase, unsigned voff, unsigned lds_dst) {
  asm volatile("s_mov_b32 m0, %2\n\ts_mov_b32 exec_hi, 0\n\tglobal_load_lds_dwordx4 %0, %1\n\ts_mov_b32 exec_hi, -1" ::"v"(voff), "s"(sbase), "s"(lds_dst) : "memory");
}
#define WAIT_V(n) asm volatile("s_waitcnt vmcnt(%0)" ::"i"(n) : "memory")
#define WAIT_L0() asm volatile("s_waitcnt lgkmcnt(0)" ::: "memory")
#define BAR() do { asm volatile("" ::: "memory"); __builtin_amdgcn_s_barrier(); asm volatile("" ::: "memory"); } while (0)
#define SCHED() __builtin_amdgcn_sched_barrier(0)

__device__ __forceinline__ bf16x8 tr_frag(LAS const char* p0, LAS const char* p1) {
  s16x4 a = __builtin_amdgcn_ds_read_tr16_b64_v4i16((LAS s16x4*)p0);
  s16x4 b = __builtin_amdgcn_ds_read_tr16_b64_v4i16((LAS s16x4*)p1);
  s16x8 c = __builtin_shufflevector(a, b, 0, 1, 2, 3, 4, 5, 6, 7);
  return __builtin_bit_cast(bf16x8, c);
}
__device__ __forceinline__ f32x16 mfma32(bf16x8 a, bf16x8 b, f32x16 c) { return __builtin_amdgcn_mfma_f32_32x32x16_bf16(a, b, c, 0, 0, 0); }
__device__ __forceinline__ unsigned pk2(float a, float b) {
  bf16x2 v = {(__bf16)a, (__bf16)b};
  return __builtin_bit_cast(unsigned, v);
}
__device__ __forceinline__ bf16x8 pack8(const f32x16& v, int base, float s) {
  u32x4 r;
  r[0] = pk2(v[base + 0] * s, v[base + 1] * s); r[1] = pk2(v[base + 2] * s, v[base + 3] * s);
  r[2] = pk2(v[base + 4] * s, v[base + 5] * s); r[3] = pk2(v[base + 6] * s, v[base + 7] * s);
  return __builtin_bit_cast(bf16x8, r);
}
__device__ __forceinline__ float dot2(unsigned a, unsigned b, float c) {
  return __builtin_amdgcn_fdot2_f32_bf16(__builtin_bit_cast(bf16x2, a), __builtin_bit_cast(bf16x2, b), c, false);
}
__device__ __forceinline__ void dot1s(float& acc, unsigned ones, unsigned a) { asm volatile("v_dot2c_f32_bf16 %0, %1, %2" : "+v"(acc) : "s"(ones), "v"(a)); }
__device__ __forceinline__ void dotsq(float& acc, unsigned a) { asm volatile("v_dot2c_f32_bf16 %0, %1, %1" : "+v"(acc) : "v"(a)); }
__device__ __forceinline__ float xor32_add(float v) { return v + __shfl_xor(v, 32); }

__device__ __forceinline__ void softmax16(const f32x16& p, u32x4& f0, u32x4& f1) {
  float mx = fmaxf(fmaxf(p[0], p[1]), fmaxf(p[2], p[3]));
#pragma unroll
  for (int i = 4; i < 16; i += 2) mx = fmaxf(mx, fmaxf(p[i], p[i + 1]));
  const float L2E = 1.4426950408889634f;
  const float t = -mx * L2E;
  float e[16]; float s = 0.f;
#pragma unroll
  for (int i = 0; i < 16; ++i) { e[i] = __builtin_amdgcn_exp2f(__builtin_fmaf(p[i], L2E, t)); s += e[i]; }
  const float r = __builtin_amdgcn_rcpf(s);
  f0[0] = pk2(e[0] * r, e[1] * r); f0[1] = pk2(e[2] * r, e[3] * r); f0[2] = pk2(e[4] * r, e[5] * r); f0[3] = pk2(e[6] * r, e[7] * r);
  f1[0] = pk2(e[8] * r, e[9] * r); f1[1] = pk2(e[10] * r, e[11] * r); f1[2] = pk2(e[12] * r, e[13] * r); f1[3] = pk2(e[14] * r, e[15] * r);
}

constexpr int P1_SLOT = 45056;
constexpr int P1_XOFF = 28672;
constexpr int P1_EX = 2 * P1_SLOT;
constexpr int P1_EX_CS = P1_EX;
constexpr int P1_EX_ST = P1_EX + 8192;
constexpr int P1_EX_T6 = P1_EX + 10240;
constexpr int P1_WPHI = 3 * P1_SLOT;
constexpr int P1_ONES = P1_WPHI + 2048;
constexpr int P1_LDS = P1_ONES + 64;

template <bool TA> struct Frags { bf16x8 x; bf16x8 w[3]; bf16x8 w6; };

template <bool TA>
__device__ __forceinline__ void phase1_body(LAS char* lds, unsigned lds0, int panel, int role, int kstart, int bid, const u16* __restrict__ x,
                                            const u16* __restrict__ wqkv, const u16* __restrict__ wphi, const u16* __restrict__ w1,
                                            const u16* __restrict__ b1, const u16* __restrict__ wo, const u16* __restrict__ w2, char* __restrict__ ws) {
  constexpr int ROWB = TA ? 384 : 448;
  constexpr int NCH = ROWB / 16;
  constexpr int NP = TA ? 5 : 6;
  constexpr int WROW = TA ? 3072 : 3584;
  const int tid = threadIdx.x, l = tid & 63, w = __builtin_amdgcn_readfirstlane(tid >> 6);
  const int tg = w & 3, hf = w >> 2;
  const int t = l & 31, hh = l >> 5;
  const int rot = (tg + 2 * hf) & 3;
  const int wtu = bid * 8 + w;
  u16 wtv[8], wt9;
  {
    const int s = wtu >> 4, nt = wtu & 15;
    const u16* src = (s < 112 ? w2 + (size_t)s * 16 * 512 : wo + (size_t)(s - 112) * 16 * 512) + nt * 32 + t + hh * 4 * 512;
#pragma unroll
    for (int j = 0; j < 8; ++j) wtv[j] = src[((j & 3) + 8 * (j >> 2)) * 512];
    wt9 = wo[(size_t)((16 + (bid >> 4)) * 16 + (w & 3) + 8 * (w >> 2) + 4 * hh) * 512 + (bid & 15) * 32 + t];
  }
  u16 b1v[4];
  if (!TA) {
#pragma unroll
    for (int j = 0; j < 3; ++j) b1v[j] = b1[role * 224 + 32 * (3 * hf + j) + t];
    b1v[3] = b1[role * 224 + 192 + (t ^ (16 * hf))];
  }
  unsigned woff[4];
#pragma unroll
  for (int i = 0; i < 4; ++i) {
    const int n = 64 * i + l, row = n / NCH, c = n % NCH;
    if (TA) { const int cl = c ^ (((row >> 1) & 1) << 2); woff[i] = row * WROW + (cl >> 3) * 1024 + role * 128 + (cl & 7) * 16; }
    else woff[i] = row * WROW + role * 448 + c * 16;
  }
  unsigned xoff[2];
#pragma unroll
  for (int i = 0; i < 2; ++i) { const int row = 8 * i + (l >> 3), c = (l & 7) ^ ((row >> 1) & 7); xoff[i] = row * 1024 + c * 16; }
  const char* wbase = (const char*)(TA ? wqkv : w1) + (size_t)(8 * w) * WROW;
  const char* xbase = (const char*)x + (size_t)(panel * 128 + 16 * w) * 1024;
  const unsigned wdst = lds0 + w * 8 * ROWB, xdst = lds0 + P1_XOFF + w * 2048;
  auto issue0 = [&](int j, unsigned slot) {
    const int kc = (kstart + j) & 7;
    const char* wb = wbase + (size_t)kc * 64 * WROW;
    glds16(wb, woff[0], wdst + slot); glds16(wb, woff[1], wdst + slot + 1024); glds16(wb, woff[2], wdst + slot + 2048);
  };
  auto issue1 = [&](int j, unsigned slot) {
    const int kc = (kstart + j) & 7;
    if (!TA) glds16_lo(wbase + (size_t)kc * 64 * WROW, woff[3], wdst + slot + 3072);
    const char* xb = xbase + kc * 128;
    glds16(xb, xoff[0], xdst + slot); glds16(xb, xoff[1], xdst + slot + 1024);
  };
  if (TA) {
    if (w == 0) { glds16((const char*)wphi + role * 2048, l * 16, lds0 + P1_WPHI); glds16((const char*)wphi + role * 2048 + 1024, l * 16, lds0 + P1_WPHI + 1024); }
    if (tid < 4) *(LAS unsigned*)(lds + P1_ONES + tid * 4) = 0x3F803F80u;
  }
  issue0(0, 0); issue1(0, 0);
  issue0(1, P1_SLOT); issue1(1, P1_SLOT);
  issue0(2, 2 * P1_SLOT);
  const int blk = (l >> 4) & 1, q = (l & 15) >> 2, p = l & 3;
  const int sw = TA ? ((q >> 1) & 1) : 0;
  const int wlane = (8 * hh + q) * ROWB + 8 * p + (TA ? (32 * blk + 64 * (hf ^ sw)) : (32 * blk + 192 * hf));
  const int w6lane = (8 * hh + q) * ROWB + 8 * p + 32 * (blk ^ hf) + 384;
  int wad[4], xad[4];
#pragma unroll
  for (int i = 0; i < 4; ++i) {
    const int ks = (rot + i) & 3;
    wad[i] = ks * 16 * ROWB;
    xad[i] = P1_XOFF + tg * 4096 + t * 128 + (((2 * ks + hh) ^ ((t >> 1) & 7)) << 4);
  }
  constexpr int TS = TA ? 128 : 64;
  auto rd = [&](Frags<TA>& f, int i, int slot, bool six) {
    f.x = *(LAS const bf16x8*)(lds + slot + xad[i]);
    LAS const char* a = lds + slot + wad[i] + wlane;
#pragma unroll
    for (int j = 0; j < 3; ++j) f.w[j] = tr_frag(a + TS * j, a + TS * j + 4 * ROWB);
    if (!TA && six) { LAS const char* a6 = lds + slot + wad[i] + w6lane; f.w6 = tr_frag(a6, a6 + 4 * ROWB); }
  };
  f32x16 acc[3], acc6;
#pragma unroll
  for (int i = 0; i < 3; ++i) acc[i] = (f32x16){};
  acc6 = (f32x16){};
  float csp[3] = {0.f, 0.f, 0.f}, cs6 = 0.f;
  float sum = 0.f, sq = 0.f;
  const unsigned ONE2 = 0x3F803F80u;
  auto mm = [&](const Frags<TA>& f, bool stats, bool cs, bool six, bool c6) {
    const u32x4 xu = __builtin_bit_cast(u32x4, f.x);
#pragma unroll
    for (int j = 0; j < 3; ++j) {
      acc[j] = mfma32(f.w[j], f.x, acc[j]);
      SCHED();
      if (cs) {
        const u32x4 wu = __builtin_bit_cast(u32x4, f.w[j]);
#pragma unroll
        for (int e = 0; e < 4; ++e) dot1s(csp[j], ONE2, wu[e]);
      }
      if (stats && j < 2) {
#pragma unroll
        for (int e = 2 * j; e < 2 * j + 2; ++e) { dot1s(sum, ONE2, xu[e]); dotsq(sq, xu[e]); }
      }
      if (!TA && c6 && j == 2) {
        const u32x4 wu = __builtin_bit_cast(u32x4, f.w6);
#pragma unroll
        for (int e = 0; e < 4; ++e) dot1s(cs6, ONE2, wu[e]);
      }
      SCHED();
    }
    if (!TA && six) acc6 = mfma32(f.w6, f.x, acc6);
  };
  Frags<TA> f0, f1;
  WAIT_V(NP + 3);
  BAR();
  rd(f0, 0, 0, true);
  int cur = 0, nxt = P1_SLOT, prv = 2 * P1_SLOT;
#pragma unroll 1
  for (int j = 0; j < 8; ++j) {
    rd(f1, 1, cur, true);
    SCHED();
    mm(f0, true, false, true, true);
    SCHED();
    rd(f0, 2, cur, false);
    if (j < 6) issue1(j + 2, prv);
    SCHED();
    mm(f1, true, false, true, false);
    SCHED();
    rd(f1, 3, cur, false);
    SCHED();
    mm(f0, false, true, false, false);
    SCHED();
    WAIT_L0();
    WAIT_V(NP);
    if (j >= 6) WAIT_V(0);
    BAR();
    rd(f0, 0, nxt, true);
    if (j < 5) issue0(j + 3, cur);
    SCHED();
    mm(f1, false, false, false, false);
    SCHED();
    const int o = cur; cur = nxt; nxt = prv; prv = o;
  }
  sum = xor32_add(sum); sq = xor32_add(sq);
  *(LAS f32x2*)(lds + P1_EX_ST + (w * 32 + t) * 8) = (f32x2){sum, sq};
#pragma unroll
  for (int j = 0; j < 3; ++j) *(LAS float*)(lds + P1_EX_CS + ((3 * hf + j) * 64 + l) * 16 + tg * 4) = csp[j];
  if (!TA) {
    *(LAS float*)(lds + P1_EX_CS + ((6 + hf) * 64 + l) * 16 + tg * 4) = cs6;
    *(LAS f32x4*)(lds + P1_EX_T6 + w * 2048 + l * 16) = (f32x4){acc6[8], acc6[9], acc6[10], acc6[11]};
    *(LAS f32x4*)(lds + P1_EX_T6 + w * 2048 + 1024 + l * 16) = (f32x4){acc6[12], acc6[13], acc6[14], acc6[15]};
  }
  WAIT_L0();
  BAR();
  {
    const f32x2 o = *(LAS const f32x2*)(lds + P1_EX_ST + ((w ^ 4) * 32 + t) * 8);
    sum += o[0]; sq += o[1];
  }
  const float mean = sum * (1.f / 512.f);
  const float var = fmaxf(sq * (1.f / 512.f) - mean * mean, 0.f) + 1e-5f;
  const float rstd = __builtin_amdgcn_rsqf(var);
#pragma unroll
  for (int j = 0; j < 3; ++j) {
    const f32x4 c4 = *(LAS const f32x4*)(lds + P1_EX_CS + ((3 * hf + j) * 64 + l) * 16);
    float cs = (c4[0] + c4[1]) + (c4[2] + c4[3]);
    if (TA) acc[j] = __builtin_amdgcn_mfma_f32_32x32x2f32(cs, -mean, acc[j], 0, 0, 0);
    else {
      cs = xor32_add(cs);
      const float bb = __builtin_bit_cast(float, (unsigned)b1v[j] << 16);
      acc[j] = __builtin_amdgcn_mfma_f32_32x32x2f32(hh ? bb : cs, hh ? var * rstd : -mean, acc[j], 0, 0, 0);
    }
  }
  const int tok32 = panel * 4 + tg;
  if (!TA) {
    {
      const f32x4 c4 = *(LAS const f32x4*)(lds + P1_EX_CS + (6 * 64 + (l ^ (16 * hf))) * 16);
      float cs = (c4[0] + c4[1]) + (c4[2] + c4[3]);
      cs = xor32_add(cs);
      const f32x4 pa = *(LAS const f32x4*)(lds + P1_EX_T6 + (w ^ 4) * 2048 + l * 16);
      const f32x4 pb = *(LAS const f32x4*)(lds + P1_EX_T6 + (w ^ 4) * 2048 + 1024 + l * 16);
#pragma unroll
      for (int e = 0; e < 4; ++e) { acc6[e] += pa[e]; acc6[4 + e] += pb[e]; }
      const float bb = __builtin_bit_cast(float, (unsigned)b1v[3] << 16);
      acc6 = __builtin_amdgcn_mfma_f32_32x32x2f32(hh ? bb : cs, hh ? var * rstd : -mean, acc6, 0, 0, 0);
    }
    char* hfp = ws + WS_HF + ((size_t)tok32 * 112 + role * 14) * 1024 + l * 16;
#pragma unroll
    for (int j = 0; j < 3; ++j) {
      u32x4 o[2];
#pragma unroll
      for (int e = 0; e < 8; ++e) {
        const float a = fmaxf(acc[j][2 * e] * rstd, 0.f), b = fmaxf(acc[j][2 * e + 1] * rstd, 0.f);
        o[e >> 2][e & 3] = pk2(a * a, b * b);
      }
      *(u32x4*)(hfp + (6 * hf + 2 * j) * 1024) = o[0];
      *(u32x4*)(hfp + (6 * hf + 2 * j + 1) * 1024) = o[1];
    }
    {
      u32x4 o;
#pragma unroll
      for (int e = 0; e < 4; ++e) {
        const float a = fmaxf(acc6[2 * e] * rstd, 0.f), b = fmaxf(acc6[2 * e + 1] * rstd, 0.f);
        o[e] = pk2(a * a, b * b);
      }
      *(u32x4*)(hfp + (12 + hf) * 1024) = o;
    }
  } else {
    const unsigned sgn = ((t >> 2) & 1) ? 0x80008000u : 0u;
    bf16x8 aphi[2];
#pragma unroll
    for (int e = 0; e < 2; ++e) {
      LAS const char* a = lds + P1_WPHI + (2 * hf + e) * 512 + (4 * hh + q) * 32 + (8 * blk + 4 * (p >> 1)) * 2;
      u32x4 u = __builtin_bit_cast(u32x4, tr_frag(a, a + 8 * 32));
      u[0] ^= sgn; u[1] ^= sgn; u[2] ^= sgn; u[3] ^= sgn;
      aphi[e] = __builtin_bit_cast(bf16x8, u);
    }
    char* pq = ws + WS_PQ + ((size_t)tok32 * 32 + role * 4 + 2 * hf) * 2048 + l * 16;
    LAS char* PK = lds;
    LAS char* VI = lds + 32768;
    const int tok = 32 * tg + t, xs = (tok >> 1) & 3;
#pragma unroll
    for (int e = 0; e < 2; ++e) {
      const bf16x8 kf = pack8(acc[1], 8 * e, rstd);
      const f32x16 pp = mfma32(aphi[e], kf, (f32x16){});
      u32x4 g0, g1; softmax16(pp, g0, g1);
      LAS char* r = PK + (2 * hf + e) * 8192 + tok * 64;
      *(LAS u32x4*)(r + (((2 * hh) ^ xs) << 4)) = g0;
      *(LAS u32x4*)(r + (((2 * hh + 1) ^ xs) << 4)) = g1;
    }
    {
      LAS char* r = VI + hf * 8192 + tok * 64 + 8 * hh;
#pragma unroll
      for (int g = 0; g < 4; ++g) {
        u32x2 o;
        o[0] = pk2(acc[2][4 * g] * rstd, acc[2][4 * g + 1] * rstd);
        o[1] = pk2(acc[2][4 * g + 2] * rstd, acc[2][4 * g + 3] * rstd);
        *(LAS u32x2*)(r + ((g ^ xs) << 4)) = o;
      }
    }
    WAIT_L0();
    BAR();
#pragma unroll
    for (int e = 0; e < 2; ++e) {
      const bf16x8 qf = pack8(acc[0], 8 * e, rstd);
      const f32x16 pp = mfma32(aphi[e], qf, (f32x16){});
      u32x4 g0, g1; softmax16(pp, g0, g1);
      *(u32x4*)(pq + e * 2048) = g0;
      *(u32x4*)(pq + e * 2048 + 1024) = g1;
    }
    if (hf == 1) {
      f32x16 kv = (f32x16){};
#pragma unroll
      for (int u = 0; u < 8; ++u) {
        const int r0 = 16 * u + 4 * hh + q, r1 = r0 + 8;
        const int ca = 2 * blk + (p >> 1);
        LAS const char* a0 = PK + tg * 8192 + r0 * 64 + ((ca ^ ((r0 >> 1) & 3)) << 4) + 8 * (p & 1);
        LAS const char* a1 = PK + tg * 8192 + r1 * 64 + ((ca ^ ((r1 >> 1) & 3)) << 4) + 8 * (p & 1);
        const int cb = 2 * (tg & 1) + (p >> 1);
        LAS const char* b0 = blk ? (lds + P1_ONES) : (VI + (tg >> 1) * 8192 + r0 * 64 + ((cb ^ ((r0 >> 1) & 3)) << 4) + 8 * (p & 1));
        LAS const char* b1p = blk ? (lds + P1_ONES) : (VI + (tg >> 1) * 8192 + r1 * 64 + ((cb ^ ((r1 >> 1) & 3)) << 4) + 8 * (p & 1));
        kv = mfma32(tr_frag(a0, a1), tr_frag(b0, b1p), kv);
      }
      if (t <= 16) {
        float* kp = (float*)(ws + WS_KVP + ((size_t)panel * 32 + role * 4 + tg) * 2176) + t * 32 + 4 * hh;
#pragma unroll
        for (int g = 0; g < 4; ++g) *(f32x4*)(kp + 8 * g) = (f32x4){kv[4 * g], kv[4 * g + 1], kv[4 * g + 2], kv[4 * g + 3]};
      }
    }
  }
  {
    u32x4 o;
#pragma unroll
    for (int e = 0; e < 4; ++e) o[e] = (unsigned)wtv[2 * e] | ((unsigned)wtv[2 * e + 1] << 16);
    *(u32x4*)(ws + WS_WT + (size_t)wtu * 1024 + l * 16) = o;
    *(u16*)(ws + WS_WT + (size_t)(2048 + bid) * 1024 + l * 16 + 2 * w) = wt9;
    if (tid == 0) *(unsigned*)(ws + WS_FLG + bid * 4) = 0u;
  }
}

__global__ __launch_bounds__(512, 2) void k_phase1(const u16* __restrict__ x, const u16* __restrict__ wqkv, const u16* __restrict__ wphi,
                                                   const u16* __restrict__ wo, const u16* __restrict__ w1, const u16* __restrict__ b1,
                                                   const u16* __restrict__ w2, char* __restrict__ ws) {
  extern __shared__ __attribute__((aligned(16))) char smem[];
  LAS char* lds = (LAS char*)smem;
  const unsigned lds0 = (unsigned)(size_t)lds;
  const int b = blockIdx.x, xcd = b & 7, idx = b >> 3;
  const int panel = (xcd & 1) * 8 + (idx & 7), cg = xcd >> 1, sel = idx >> 3;
  const int role = cg + 4 * (sel & 1);
  const int kstart = ((idx & 7) + 2 * sel) & 7;
  if (sel < 2) phase1_body<true>(lds, lds0, panel, role, kstart, b, x, wqkv, wphi, w1, b1, wo, w2, ws);
  else phase1_body<false>(lds, lds0, panel, role, kstart, b, x, wqkv, wphi, w1, b1, wo, w2, ws);
}

constexpr int P2_RS = 272;
constexpr int P2_LDS = 8 * 64 * P2_RS;
#define P2_D 6

__global__ __launch_bounds__(512, 2) void k_phase2(const u16* __restrict__ x, const u16* __restrict__ b2, char* __restrict__ ws, u16* __restrict__ out) {
  extern __shared__ __attribute__((aligned(16))) char smem[];
  LAS char* lds = (LAS char*)smem;
  const int b = blockIdx.x, xcd = b & 7, idx = b >> 3;
  const int tt = 4 * xcd + (idx >> 3), ct = idx & 7;
  const int tid = threadIdx.x, l = tid & 63, w = __builtin_amdgcn_readfirstlane(tid >> 6);
  const int t = l & 31, hh = l >> 5;
  f32x4 part[16];
  if (w == 0 && l < 17) {
    const float* src = (const float*)(ws + WS_KVP + (size_t)(b >> 3) * 2176) + l * 32 + (b & 7) * 4;
#pragma unroll
    for (int r = 0; r < 16; ++r) part[r] = *(const f32x4*)(src + (size_t)r * 32 * 544);
  }
  const __amdgpu_buffer_rsrc_t rs = __builtin_amdgcn_make_buffer_rsrc((void*)ws, 0, (int)WS_END, 0x00020000);
  const unsigned lo16 = l * 16;
  const unsigned wt = (unsigned)WS_WT + ((14 * w) * 16 + 2 * ct) * 1024;
  const unsigned hp = (unsigned)WS_HF + ((2 * tt) * 112 + 14 * w) * 1024;
  const unsigned wto = (unsigned)WS_WT + ((112 + 4 * w) * 16 + 2 * ct) * 1024;
  const unsigned pq = (unsigned)WS_PQ + ((2 * tt) * 32 + 4 * w) * 2048;
  auto ld = [&](unsigned so) { return __builtin_bit_cast(bf16x8, __builtin_amdgcn_raw_buffer_load_b128(rs, lo16, so, 0)); };
  bf16x8 A0[14], A1[14], B0[14], B1[14];
  bf16x8 WA0[4], WA1[4], PQ[4][2][2];
  auto ldf = [&](int n) {
    A0[n] = ld(wt + n * 16384); A1[n] = ld(wt + n * 16384 + 1024);
    B0[n] = ld(hp + n * 1024); B1[n] = ld(hp + (112 + n) * 1024);
  };
  auto lda = [&](int n) {
    WA0[n] = ld(wto + n * 16384); WA1[n] = ld(wto + n * 16384 + 1024);
#pragma unroll
    for (int j = 0; j < 2; ++j) { PQ[n][j][0] = ld(pq + (32 * j + n) * 2048); PQ[n][j][1] = ld(pq + (32 * j + n) * 2048 + 1024); }
  };
#pragma unroll
  for (int n = 0; n < P2_D; ++n) ldf(n);
  SCHED();
  if (w == 0 && l < 17) {
    f32x4 a = part[0];
#pragma unroll
    for (int r = 1; r < 16; ++r) a += part[r];
    const unsigned long long o = (unsigned long long)pk2(a[0], a[1]) | ((unsigned long long)pk2(a[2], a[3]) << 32);
    __hip_atomic_store((gu64*)(ws + WS_KVZ + ((size_t)((b >> 3) * 4 + ((b & 7) >> 1)) * 17 + l) * 16 + (b & 1) * 8), o, RLX_AGENT);
    WAIT_V(0);
    if (l == 0) __hip_atomic_store((gu32*)(ws + WS_FLG + b * 4), 1u, RLX_AGENT);
  }
  SCHED();
  f32x16 acc[2][2];
#pragma unroll
  for (int i = 0; i < 2; ++i)
#pragma unroll
    for (int j = 0; j < 2; ++j) acc[i][j] = (f32x16){};
  gu32* flg = (gu32*)(ws + WS_FLG + w * 128 + (l & 31) * 4);
  unsigned fl = 0;
  u32x4 KZ[4][2];
  const int tokr = tid >> 3, n0 = (tid & 7) * 8;
  const size_t go = (size_t)(tt * 64 + tokr) * 512 + ct * 64 + n0;
  u32x4 xa, ba;
#pragma unroll
  for (int n = 0; n < 14; ++n) {
    if (n + P2_D < 14) ldf(n + P2_D);
    else if (n + P2_D - 14 < 4) lda(n + P2_D - 14);
    if (n == 5) fl = __hip_atomic_load(flg, RLX_AGENT);
    if (n == 8) {
      while (__builtin_amdgcn_ballot_w64(fl != 1u) != 0ull) { __builtin_amdgcn_s_sleep(2); fl = __hip_atomic_load(flg, RLX_AGENT); }
      const unsigned kzo = (t < 16 ? t : 16) * 16 + hh * 2 * 272;
#pragma unroll
      for (int hd = 0; hd < 4; ++hd)
#pragma unroll
        for (int e = 0; e < 2; ++e)
          KZ[hd][e] = __builtin_bit_cast(u32x4, __builtin_amdgcn_raw_buffer_load_b128(rs, kzo, (unsigned)WS_KVZ + (4 * w + hd) * 1088 + e * 272, 16));
    }
    if (n == 12) { xa = *(const u32x4*)(x + go); ba = *(const u32x4*)(b2 + ct * 64 + n0); }
    SCHED();
    acc[0][0] = mfma32(A0[n], B0[n], acc[0][0]);
    acc[0][1] = mfma32(A0[n], B1[n], acc[0][1]);
    acc[1][0] = mfma32(A1[n], B0[n], acc[1][0]);
    acc[1][1] = mfma32(A1[n], B1[n], acc[1][1]);
    SCHED();
  }
#pragma unroll
  for (int hd = 0; hd < 4; ++hd) {
    if (14 + hd + P2_D - 14 < 4) lda(hd + P2_D);
    SCHED();
#pragma unroll
    for (int j = 0; j < 2; ++j) {
      f32x16 nm = mfma32(__builtin_bit_cast(bf16x8, KZ[hd][0]), PQ[hd][j][0], (f32x16){});
      nm = mfma32(__builtin_bit_cast(bf16x8, KZ[hd][1]), PQ[hd][j][1], nm);
      const float rd = __builtin_amdgcn_rcpf(nm[8] + 1e-6f);
      const bf16x8 of = pack8(nm, 0, rd);
      acc[0][j] = mfma32(WA0[hd], of, acc[0][j]);
      acc[1][j] = mfma32(WA1[hd], of, acc[1][j]);
    }
    SCHED();
  }
#pragma unroll
  for (int i = 0; i < 2; ++i)
#pragma unroll
    for (int j = 0; j < 2; ++j)
#pragma unroll
      for (int g = 0; g < 4; ++g)
        *(LAS f32x4*)(lds + w * (64 * P2_RS) + (32 * j + t) * P2_RS + (32 * i + 8 * g + 4 * hh) * 4) =
            (f32x4){acc[i][j][4 * g], acc[i][j][4 * g + 1], acc[i][j][4 * g + 2], acc[i][j][4 * g + 3]};
  WAIT_L0();
  BAR();
  {
    f32x4 s0 = *(LAS const f32x4*)(lds + tokr * P2_RS + n0 * 4), s1 = *(LAS const f32x4*)(lds + tokr * P2_RS + n0 * 4 + 16);
#pragma unroll
    for (int ww = 1; ww < 8; ++ww) {
      s0 += *(LAS const f32x4*)(lds + ww * (64 * P2_RS) + tokr * P2_RS + n0 * 4);
      s1 += *(LAS const f32x4*)(lds + ww * (64 * P2_RS) + tokr * P2_RS + n0 * 4 + 16);
    }
    auto lo = [](unsigned u) { return __builtin_bit_cast(float, u << 16); };
    auto hi = [](unsigned u) { return __builtin_bit_cast(float, u & 0xffff0000u); };
    u32x4 o;
    o[0] = pk2(s0[0] + lo(xa[0]) + lo(ba[0]), s0[1] + hi(xa[0]) + hi(ba[0]));
    o[1] = pk2(s0[2] + lo(xa[1]) + lo(ba[1]), s0[3] + hi(xa[1]) + hi(ba[1]));
    o[2] = pk2(s1[0] + lo(xa[2]) + lo(ba[2]), s1[1] + hi(xa[2]) + hi(ba[2]));
    o[3] = pk2(s1[2] + lo(xa[3]) + lo(ba[3]), s1[3] + hi(xa[3]) + hi(ba[3]));
    *(u32x4*)(out + go) = o;
  }
}

extern "C" void kernel_launch(void* const* d_in, const int* in_sizes, int n_in, void* d_out, int out_size, void* d_ws, size_t ws_size, hipStream_t stream) {
  const u16* x = (const u16*)d_in[0];
  const u16* wqkv = (const u16*)d_in[1];
  const u16* wphi = (const u16*)d_in[2];
  const u16* wo = (const u16*)d_in[3];
  const u16* w1 = (const u16*)d_in[5];
  const u16* b1 = (const u16*)d_in[6];
  const u16* w2 = (const u16*)d_in[7];
  const u16* b2 = (const u16*)d_in[8];
  char* ws = (char*)d_ws;
  hipFuncSetAttribute((const void*)k_phase1, hipFuncAttributeMaxDynamicSharedMemorySize, P1_LDS);
  hipFuncSetAttribute((const void*)k_phase2, hipFuncAttributeMaxDynamicSharedMemorySize, P2_LDS);
  k_phase1<<<256, 512, P1_LDS, stream>>>(x, wqkv, wphi, wo, w1, b1, w2, ws);
  k_phase2<<<256, 512, P2_LDS, stream>>>(x, b2, ws, (u16*)d_out);
}
```

```cpp
#include <hip/hip_runtime.h>
#include <hip/hip_bf16.h>
#include <stdint.h>

#define LAS __attribute__((address_space(3)))
#define GAS __attribute__((address_space(1)))
typedef __bf16 bf16x8 __attribute__((ext_vector_type(8)));
typedef __bf16 bf16x2 __attribute__((ext_vector_type(2)));
typedef short s16x4 __attribute__((ext_vector_type(4)));
typedef short s16x8 __attribute__((ext_vector_type(8)));
typedef float f32x16 __attribute__((ext_vector_type(16)));
typedef float f32x4 __attribute__((ext_vector_type(4)));
typedef float f32x2 __attribute__((ext_vector_type(2)));
typedef unsigned u32x4 __attribute__((ext_vector_type(4)));
typedef unsigned u32x2 __attribute__((ext_vector_type(2)));
typedef unsigned short u16;
typedef GAS unsigned gu32;
#define RLX_AGENT __ATOMIC_RELAXED, __HIP_MEMORY_SCOPE_AGENT

constexpr unsigned WS_E = 0;
constexpr unsigned WS_FH = 1024;
constexpr unsigned WS_FW = WS_FH + 32768;
constexpr unsigned WS_FK = WS_FW + 8192;
constexpr unsigned WS_ST = WS_FK + 1024;
constexpr unsigned WS_CS = WS_ST + 32768;
constexpr unsigned WS_KVZ = WS_CS + 53248;
constexpr unsigned WS_KVP = WS_KVZ + 34816;
constexpr unsigned WS_WT = WS_KVP + (4u << 20);
constexpr unsigned WS_HF = WS_WT + 8 * 144 * 2048;
constexpr unsigned WS_PQ = WS_HF + 64 * 112 * 1024;
constexpr unsigned WS_END = WS_PQ + 64 * 32 * 2048;

template <int N> struct IC { static constexpr int v = N; };
template <int I, int N, class F> __device__ __forceinline__ void sfor(F&& f) { if constexpr (I < N) { f(IC<I>{}); sfor<I + 1, N>(f); } }

__device__ __forceinline__ void glds16(const void* sbase, unsigned voff, unsigned lds_dst) {
  asm volatile("s_mov_b32 m0, %2\n\ts_nop 0\n\tglobal_load_lds_dwordx4 %0, %1" ::"v"(voff), "s"(sbase), "s"(lds_dst) : "memory");
}
__device__ __forceinline__ void glds16_lo(const void* sbase, unsigned voff, unsigned lds_dst) {
  asm volatile("s_mov_b32 m0, %2\n\ts_mov_b32 exec_hi, 0\n\tglobal_load_lds_dwordx4 %0, %1\n\ts_mov_b32 exec_hi, -1" ::"v"(voff), "s"(sbase), "s"(lds_dst) : "memory");
}
template <int OFF> __device__ __forceinline__ void ald4(u32x4& d, const void* sb, unsigned vo) {
  asm volatile("global_load_dwordx4 %0, %1, %2 offset:%3 ; ALD" : "=v"(d) : "v"(vo), "s"(sb), "i"(OFF) : "memory");
}
template <int OFF> __device__ __forceinline__ void ald4c(u32x4& d, const void* sb, unsigned vo) {
  asm volatile("global_load_dwordx4 %0, %1, %2 offset:%3 sc1 ; ALD" : "=v"(d) : "v"(vo), "s"(sb), "i"(OFF) : "memory");
}
template <int OFF> __device__ __forceinline__ void ald2c(u32x2& d, const void* sb, unsigned vo) {
  asm volatile("global_load_dwordx2 %0, %1, %2 offset:%3 sc1 ; ALD" : "=v"(d) : "v"(vo), "s"(sb), "i"(OFF) : "memory");
}
template <int OFF> __device__ __forceinline__ void ald1c(unsigned& d, const void* sb, unsigned vo) {
  asm volatile("global_load_dword %0, %1, %2 offset:%3 sc1 ; ALD" : "=v"(d) : "v"(vo), "s"(sb), "i"(OFF) : "memory");
}
template <int OFF> __device__ __forceinline__ void ald1(unsigned& d, const void* sb, unsigned vo) {
  asm volatile("global_load_dword %0, %1, %2 offset:%3 ; ALD" : "=v"(d) : "v"(vo), "s"(sb), "i"(OFF) : "memory");
}
__device__ __forceinline__ void aldh(unsigned& d, const void* sb, unsigned vo) {
  asm volatile("global_load_ushort %0, %1, %2 ; ALD" : "=v"(d) : "v"(vo), "s"(sb) : "memory");
}
#define TIE(x) asm volatile("; TIE %0" : "+v"(x))
template <int OFF> __device__ __forceinline__ void st4(const void* sb, unsigned vo, u32x4 v) {
  asm volatile("global_store_dwordx4 %0, %1, %2 offset:%3 sc1\n\ts_nop 1" ::"v"(vo), "v"(v), "s"(sb), "i"(OFF) : "memory");
}
__device__ __forceinline__ void st1(const void* sb, unsigned vo, unsigned v) {
  asm volatile("global_store_dword %0, %1, %2 sc1" ::"v"(vo), "v"(v), "s"(sb) : "memory");
}
__device__ __forceinline__ void st4_m(const void* sb, unsigned vo, u32x4 v, unsigned long long mask) {
  asm volatile("s_mov_b64 exec, %3\n\tglobal_store_dwordx4 %0, %1, %2 sc1\n\ts_mov_b64 exec, -1\n\ts_nop 0" ::"v"(vo), "v"(v), "s"(sb), "s"(mask) : "memory");
}
__device__ __forceinline__ void st1_m(const void* sb, unsigned vo, unsigned v, unsigned long long mask) {
  asm volatile("s_mov_b64 exec, %3\n\tglobal_store_dword %0, %1, %2 sc1\n\ts_mov_b64 exec, -1" ::"v"(vo), "v"(v), "s"(sb), "s"(mask) : "memory");
}
#define WAIT_V(n) asm volatile("s_waitcnt vmcnt(%0)" ::"i"(n) : "memory")
#define WAIT_L0() asm volatile("s_waitcnt lgkmcnt(0)" ::: "memory")
#define BAR() do { asm volatile("" ::: "memory"); __builtin_amdgcn_s_barrier(); asm volatile("" ::: "memory"); } while (0)
#define SCHED() __builtin_amdgcn_sched_barrier(0)
#define TS_DECL
#define TS(i)
#define TS_OUT()

__device__ __forceinline__ bf16x8 tr_frag(unsigned a0, unsigned a1) {
  s16x4 a = __builtin_amdgcn_ds_read_tr16_b64_v4i16((LAS s16x4*)(size_t)a0);
  s16x4 b = __builtin_amdgcn_ds_read_tr16_b64_v4i16((LAS s16x4*)(size_t)a1);
  s16x8 c = __builtin_shufflevector(a, b, 0, 1, 2, 3, 4, 5, 6, 7);
  return __builtin_bit_cast(bf16x8, c);
}
__device__ __forceinline__ f32x16 mfma32(bf16x8 a, bf16x8 b, f32x16 c) { return __builtin_amdgcn_mfma_f32_32x32x16_bf16(a, b, c, 0, 0, 0); }
__device__ __forceinline__ f32x16 mfma32u(u32x4 a, u32x4 b, f32x16 c) { return mfma32(__builtin_bit_cast(bf16x8, a), __builtin_bit_cast(bf16x8, b), c); }
__device__ __forceinline__ unsigned pk2(float a, float b) {
  bf16x2 v = {(__bf16)a, (__bf16)b};
  return __builtin_bit_cast(unsigned, v);
}
__device__ __forceinline__ bf16x8 pack8(const f32x16& v, int base, float s) {
  u32x4 r;
  r[0] = pk2(v[base + 0] * s, v[base + 1] * s); r[1] = pk2(v[base + 2] * s, v[base + 3] * s);
  r[2] = pk2(v[base + 4] * s, v[base + 5] * s); r[3] = pk2(v[base + 6] * s, v[base + 7] * s);
  return __builtin_bit_cast(bf16x8, r);
}
__device__ __forceinline__ float asf(unsigned u) { return __builtin_bit_cast(float, u); }
__device__ __forceinline__ unsigned asu(float f) { return __builtin_bit_cast(unsigned, f); }
__device__ __forceinline__ float blo(unsigned u) { return asf(u << 16); }
__device__ __forceinline__ float bhi(unsigned u) { return asf(u & 0xffff0000u); }
template <int CTRL> __device__ __forceinline__ float dpp_add(float v) { return v + asf((unsigned)__builtin_amdgcn_update_dpp(0, (int)asu(v), CTRL, 0xf, 0xf, false)); }
__device__ __forceinline__ float sw16_add(float a, float b) {
  auto r = __builtin_amdgcn_permlane16_swap(asu(a), asu(b), false, false); return asf(r[0]) + asf(r[1]);
}
__device__ __forceinline__ float sw32_add(float a, float b) {
  auto r = __builtin_amdgcn_permlane32_swap(asu(a), asu(b), false, false); return asf(r[0]) + asf(r[1]);
}
__device__ __forceinline__ float wave_sum(float v) {
  v = dpp_add<0x128>(v); v = dpp_add<0x124>(v); v = dpp_add<0x122>(v); v = dpp_add<0x121>(v);
  v = sw16_add(v, v); v = sw32_add(v, v);
  return v;
}
__device__ __forceinline__ bool any_set(bool c) { return __builtin_amdgcn_ballot_w64(c) != 0ull; }

__device__ __forceinline__ void softmax16(const f32x16& p, u32x4& f0, u32x4& f1) {
  float mx = fmaxf(fmaxf(p[0], p[1]), p[2]);
#pragma unroll
  for (int i = 3; i < 15; i += 2) mx = fmaxf(fmaxf(mx, p[i]), p[i + 1]);
  mx = fmaxf(mx, p[15]);
  float e[16]; float s = 0.f;
#pragma unroll
  for (int i = 0; i < 16; ++i) { e[i] = __builtin_amdgcn_exp2f(p[i] - mx); s += e[i]; }
  const float r = __builtin_amdgcn_rcpf(s);
  f0[0] = pk2(e[0] * r, e[1] * r); f0[1] = pk2(e[2] * r, e[3] * r); f0[2] = pk2(e[4] * r, e[5] * r); f0[3] = pk2(e[6] * r, e[7] * r);
  f1[0] = pk2(e[8] * r, e[9] * r); f1[1] = pk2(e[10] * r, e[11] * r); f1[2] = pk2(e[12] * r, e[13] * r); f1[3] = pk2(e[14] * r, e[15] * r);
}

constexpr int L_XS = 16384;
constexpr int L_W = 49152, L_WS = 28672;
constexpr int L_WPHI = 135168;
constexpr int L_ONES = L_WPHI + 2048;
constexpr int L_CSX = L_ONES + 64;
constexpr int L_PK = 32768;
constexpr int L_VI = 0;
constexpr int L_T6 = 0;
constexpr int P2_RS = 272;
constexpr int LDS_BYTES = 8 * 64 * P2_RS;
constexpr int SPIN_CAP = 20000;
#define NW5 6
#define NW6 7
#define NW7 7
#define P2_D 7

struct Fr { bf16x8 x, w0, w1, w2, w6; };

template <bool TA>
__device__ __forceinline__ void body(unsigned lds0, int b, const u16* __restrict__ x, const u16* __restrict__ wqkv, const u16* __restrict__ wphi,
                                     const u16* __restrict__ wo, const u16* __restrict__ w1, const u16* __restrict__ b1, const u16* __restrict__ w2,
                                     const u16* __restrict__ b2, char* __restrict__ ws, u16* __restrict__ out) {
  constexpr int ROWB = TA ? 384 : 448;
  constexpr int NCH = ROWB / 16;
  constexpr int NP = TA ? 5 : 6;
  constexpr int WROW = TA ? 3072 : 3584;
  constexpr int NT = TA ? 3 : 4;
  constexpr int G = NT + 2;
  TS_DECL; TS(0);
  const int xcd = b & 7, idx = b >> 3;
  const int panel = (xcd & 1) * 8 + (idx & 7), sel = idx >> 3, role = (xcd >> 1) + 4 * (sel & 1);
  const int kstart = ((idx & 7) + 2 * sel) & 7;
  const int tt = 4 * xcd + sel, ct = idx & 7;
  int tid = threadIdx.x;
  asm volatile("; body %1" : "+v"(tid) : "i"((int)TA));
  const int l = tid & 63, w = __builtin_amdgcn_readfirstlane(tid >> 6);
  const int tg = w & 3, hf = w >> 2;
  const int t = l & 31, hh = l >> 5;
  unsigned E;
  asm volatile("s_load_dword %0, %1, 0x0" : "=s"(E) : "s"(ws) : "memory");
  unsigned woff[4];
#pragma unroll
  for (int i = 0; i < 4; ++i) {
    const int n = 64 * i + l, row = n / NCH, c = n % NCH;
    if (TA) { const int cl = c ^ (((row >> 1) & 1) << 2); woff[i] = row * WROW + (cl >> 3) * 1024 + role * 128 + (cl & 7) * 16; }
    else woff[i] = row * WROW + role * 448 + c * 16;
  }
  unsigned xoff[2];
#pragma unroll
  for (int i = 0; i < 2; ++i) { const int row = 8 * i + (l >> 3), c = (l & 7) ^ ((row >> 1) & 7); xoff[i] = row * 1024 + c * 16; }
  const char* wmat = (const char*)(TA ? wqkv : w1);
  const char* wbase = wmat + (size_t)(8 * w) * WROW;
  const char* xbase = (const char*)x + (size_t)(panel * 128 + 16 * w) * 1024;
  const unsigned wdst = lds0 + L_W + w * 8 * ROWB, xdst = lds0 + w * 2048;
  auto gw = [&](int j, int piece) {
    const int kc = (kstart + j) & 7;
    const char* wb = wbase + (size_t)kc * 64 * WROW;
    const unsigned d = wdst + (j % 3) * L_WS + piece * 1024;
    if (piece < 3) glds16(wb, woff[piece], d); else glds16_lo(wb, woff[3], d);
  };
  auto gx = [&](int j, int piece) {
    const int kc = (kstart + j) & 7;
    glds16(xbase + kc * 128, xoff[piece], xdst + (j % 3) * L_XS + piece * 1024);
  };
  auto issue0 = [&](int j) { gw(j, 0); gw(j, 1); gw(j, 2); };
  auto issue1 = [&](int j) { if (!TA) gw(j, 3); gx(j, 0); gx(j, 1); };
  issue0(0); issue1(0);
  u32x4 jx, jc0, jc1; unsigned jw[8], jb = 0;
  const int stok = panel * 128 + ((TA ? 0 : 8) + role) * 8 + w;
  ald4<0>(jx, (const char*)x + (size_t)stok * 1024, l * 16);
  const int wu = w * 256 + b, wuu = wu < 1152 ? wu : wu - 1152, wjs = wuu >> 3, wjc = wuu & 7;
  {
    const char* src = wjs < 112 ? (const char*)w2 + (size_t)wjs * 16384 : (const char*)wo + (size_t)(wjs - 112) * 16384;
    const unsigned vo = hh * 4096 + wjc * 128 + t * 4;
    ald1<0>(jw[0], src, vo); ald1<1024>(jw[1], src, vo); ald1<2048>(jw[2], src, vo); ald1<3072>(jw[3], src, vo);
    ald1<0>(jw[4], src + 8192, vo); ald1<1024>(jw[5], src + 8192, vo); ald1<2048>(jw[6], src + 8192, vo); ald1<3072>(jw[7], src + 8192, vo);
  }
  constexpr int NCJ = TA ? 12 : 14;
  const int cp = panel < NCJ ? panel : panel - NCJ;
  const int ccol = TA ? (cp >> 2) * 512 + role * 64 + (cp & 3) * 16 : role * 224 + cp * 16;
  {
    const unsigned vo = (64 * w + (l >> 1)) * WROW + ccol * 2 + (l & 1) * 16;
    ald4<0>(jc0, wmat, vo); ald4<0>(jc1, wmat + 32 * WROW, vo);
    if (!TA) aldh(jb, b1, (ccol + (l & 15)) * 2);
  }
  if (TA) {
    if (w == 0) { glds16((const char*)wphi + role * 2048, l * 16, lds0 + L_WPHI); glds16((const char*)wphi + role * 2048 + 1024, l * 16, lds0 + L_WPHI + 1024); }
    if (tid < 4) *(LAS unsigned*)(size_t)(lds0 + L_ONES + tid * 4) = 0x3F803F80u;
  }
  issue0(1); issue1(1);
  issue0(2);
  const int blk = (l >> 4) & 1, q = (l & 15) >> 2, p = l & 3;
  const int sw = TA ? ((q >> 1) & 1) : 0;
  const unsigned wlane = lds0 + L_W + (8 * hh + q) * ROWB + 8 * p + (TA ? (32 * blk + 64 * (hf ^ sw)) : (32 * blk + 192 * hf));
  const unsigned w6lane = lds0 + L_W + (8 * hh + q) * ROWB + 8 * p + 32 * (blk ^ hf) + 384;
  unsigned wad[4], xad[4], w6ad[2];
#pragma unroll
  for (int i = 0; i < 4; ++i) {
    const int ks = (2 * hf + i) & 3;
    wad[i] = wlane + ks * 16 * ROWB;
    xad[i] = lds0 + tg * 4096 + t * 128 + (((2 * ks + hh) ^ ((t >> 1) & 7)) << 4);
    if (i < 2) w6ad[i] = w6lane + ks * 16 * ROWB;
  }
  constexpr int TSB = TA ? 128 : 64;
  auto rdx = [&](Fr& f, int i, int slot) { f.x = *(LAS const bf16x8*)(size_t)(xad[i] + slot * L_XS); };
  auto rdw = [&](int i, int slot, int j) { const unsigned a = wad[i] + slot * L_WS + TSB * j; return tr_frag(a, a + 4 * ROWB); };
  auto rd6 = [&](int i, int slot) { const unsigned a = w6ad[i] + slot * L_WS; return tr_frag(a, a + 4 * ROWB); };
  f32x16 acc[4];
#pragma unroll
  for (int i = 0; i < 4; ++i) acc[i] = (f32x16){};
  u32x4 AW[28];
  const char* wtb = ws + WS_WT + (size_t)(ct * 144 + 14 * w) * 2048;
  const unsigned lo16 = l * 16;
  auto ldaw = [&](auto F) { constexpr int f = decltype(F)::v; ald4c<(f & 3) * 1024>(AW[f], wtb + (f >> 2) * 4096, lo16); };
  u32x4 gst; u32x2 gcs[4]; unsigned gfl;
  const int tok32 = panel * 4 + tg;
  auto ldg = [&]() {
    ald4c<0>(gst, ws + WS_ST, (tok32 * 32 + t) * 16);
#pragma unroll
    for (int j = 0; j < NT; ++j) {
      const int col = TA ? j * 512 + role * 64 + hf * 32 + t : 1536 + role * 224 + (j < 3 ? 32 * (3 * hf + j) + t : 192 + (t ^ (16 * hf)));
      ald2c<0>(gcs[j], ws + WS_CS, col * 16 + hh * 8);
    }
    ald1c<0>(gfl, ws + WS_FW + (ct * 8 + w) * 128, (t < 18 ? t : 0) * 4);
  };
  unsigned tag;
  float rstd;
  Fr f0, f1;
  TS(1);
  WAIT_V(NP + 3);
  asm volatile("s_waitcnt lgkmcnt(0)" : "+s"(E)::"memory");
  tag = E + 1;
  TIE(jx); TIE(jc0); TIE(jc1);
#pragma unroll
  for (int i = 0; i < 8; ++i) TIE(jw[i]);
  if (!TA) TIE(jb);
  BAR();
  TS(2);
  rdx(f0, 0, 0); f0.w0 = rdw(0, 0, 0); f0.w1 = rdw(0, 0, 1); f0.w2 = rdw(0, 0, 2);
  if (!TA) f0.w6 = rd6(0, 0);

  auto step = [&](Fr& fc, Fr& fn, auto I, auto NI, auto NSL, auto RD, auto&& fill) {
    constexpr int i = decltype(I)::v, ni = decltype(NI)::v, nsl = decltype(NSL)::v;
    constexpr bool rd = decltype(RD)::v != 0;
    acc[0] = mfma32(fc.w0, fc.x, acc[0]);
    SCHED();
    if (rd) { rdx(fn, ni, nsl); fn.w0 = rdw(ni, nsl, 0); }
    fill(IC<0>{});
    SCHED();
    acc[1] = mfma32(fc.w1, fc.x, acc[1]);
    SCHED();
    if (rd) { fn.w1 = rdw(ni, nsl, 1); fn.w2 = rdw(ni, nsl, 2); }
    fill(IC<1>{});
    SCHED();
    acc[2] = mfma32(fc.w2, fc.x, acc[2]);
    SCHED();
    if (!TA && rd && ni < 2) fn.w6 = rd6(ni, nsl);
    fill(IC<2>{});
    SCHED();
    if (!TA && i < 2) { acc[3] = mfma32(fc.w6, fc.x, acc[3]); SCHED(); }
  };
  sfor<0, 8>([&](auto J) {
    constexpr int j = decltype(J)::v;
    constexpr int sl = j % 3, nsl = (j + 1) % 3;
    step(f0, f1, IC<0>{}, IC<1>{}, IC<sl>{}, IC<1>{}, [&](auto S) {
      constexpr int s = decltype(S)::v;
      if (j == 0 && s == 0) {
        if (wu < 1152) {
          u32x4 o0, o1;
#pragma unroll
          for (int e = 0; e < 4; ++e) { o0[e] = __builtin_amdgcn_perm(jw[2 * e + 1], jw[2 * e], 0x05040100u); o1[e] = __builtin_amdgcn_perm(jw[2 * e + 1], jw[2 * e], 0x07060302u); }
          const char* dst = ws + WS_WT + (size_t)(wjc * 144 + wjs) * 2048;
          st4<0>(dst, lo16, o0); st4<1024>(dst, lo16, o1);
        }
      }
      if (j == 0 && s == 1) {
        float v[8];
#pragma unroll
        for (int e = 0; e < 4; ++e) { v[2 * e] = blo(jx[e]); v[2 * e + 1] = bhi(jx[e]); }
        const float sm = ((v[0] + v[1]) + (v[2] + v[3])) + ((v[4] + v[5]) + (v[6] + v[7]));
        const float mean = wave_sum(sm) * (1.f / 512.f);
        float sq = 0.f;
#pragma unroll
        for (int e = 0; e < 8; ++e) { const float d = v[e] - mean; sq = __builtin_fmaf(d, d, sq); }
        const float rs = __builtin_amdgcn_rsqf(wave_sum(sq) * (1.f / 512.f) + 1e-5f);
        st4_m(ws + WS_ST, stok * 16, (u32x4){asu(mean), tag, asu(rs), tag}, 1ull);
      }
      if (j == 4) { if (s == 0) ldg(); }
      if constexpr (j == 6 && s < 2) sfor<NW5 + 2 * s, NW5 + 2 * s + 2>(ldaw);
      if constexpr (j == 7 && s < 2) sfor<NW5 + NW6 + s, NW5 + NW6 + s + 1>(ldaw);
    });
    step(f1, f0, IC<1>{}, IC<2>{}, IC<sl>{}, IC<1>{}, [&](auto S) {
      constexpr int s = decltype(S)::v;
      if (j + 2 < 8) {
        if (s == 0) { if (!TA) gw(j + 2, 3); else gx(j + 2, 0); }
        if (s == 1) { if (!TA) gx(j + 2, 0); else gx(j + 2, 1); }
        if (s == 2) { if (!TA) gx(j + 2, 1); }
      }
      if (j == 0 && s == 2) {
        float c[8];
#pragma unroll
        for (int e = 0; e < 4; ++e) { c[2 * e] = blo(jc0[e]) + blo(jc1[e]); c[2 * e + 1] = bhi(jc0[e]) + bhi(jc1[e]); }
        float d[4], g[2];
#pragma unroll
        for (int e = 0; e < 4; ++e) d[e] = sw32_add(c[e], c[4 + e]);
#pragma unroll
        for (int e = 0; e < 2; ++e) { g[e] = sw16_add(d[e], d[2 + e]); g[e] = dpp_add<0x128>(g[e]); g[e] = dpp_add<0x124>(g[e]); g[e] = dpp_add<0x122>(g[e]); }
        if ((l & 14) == 0) {
          const int col = (l & 1) * 8 + hh * 4 + blk * 2;
          LAS float* d0 = (LAS float*)(size_t)(lds0 + L_CSX + (col * 8 + w) * 4);
          d0[0] = g[0]; d0[8] = g[1];
        }
      }
      if constexpr (j == 6 && s < 2) sfor<NW5 + 4 + s, NW5 + 4 + s + 1>(ldaw);
      if constexpr (j == 7 && s < 2) sfor<NW5 + NW6 + 2 + s, NW5 + NW6 + 2 + s + 1>(ldaw);
    });
    step(f0, f1, IC<2>{}, IC<3>{}, IC<sl>{}, IC<1>{}, [&](auto S) {
      constexpr int s = decltype(S)::v;
      if constexpr (j == 7 && s == 0) sfor<NW5 + NW6 + 4, NW5 + NW6 + 5>(ldaw);
      if constexpr (j == 7 && s == 1) sfor<NW5 + NW6 + 5, NW5 + NW6 + NW7>(ldaw);
    });
    if (j < 7) {
      WAIT_L0();
      constexpr int N = j == 0 ? NP + 1 : j == 4 ? NP + G : j == 6 ? NW5 + 6 : NP;
      WAIT_V(N);
      BAR();
    }
    step(f1, f0, IC<3>{}, IC<0>{}, IC<nsl>{}, IC<(j < 7)>{}, [&](auto S) {
      constexpr int s = decltype(S)::v;
      if constexpr (j + 3 < 8) gw(j + 3, s);
      if (j == 0 && s == 2) {
        if (w == 0 && panel < NCJ) {
          const f32x4 a = *(LAS const f32x4*)(size_t)(lds0 + L_CSX + (l & 15) * 32), c = *(LAS const f32x4*)(size_t)(lds0 + L_CSX + (l & 15) * 32 + 16);
          const float cs = ((a[0] + a[1]) + (a[2] + a[3])) + ((c[0] + c[1]) + (c[2] + c[3]));
          st4_m(ws + WS_CS, ((TA ? 0 : 1536) + ccol + (l & 15)) * 16, (u32x4){asu(cs), tag, TA ? 0u : jb << 16, tag}, 0xffffull);
        }
      }
      if (j == 1 && s == 0) {
        if (wu < 1152) {
          const int fw = wjs < 112 ? wjs / 14 : (wjs - 112) >> 2, fp = wjs < 112 ? wjs % 14 : 14 + ((wjs - 112) & 3);
          st1_m(ws + WS_FW, ((wjc * 8 + fw) * 32 + fp) * 4, tag, 1ull);
        }
      }
      if (j == 5) {
        if (s == 0) {
          TIE(gst); TIE(gfl);
#pragma unroll
          for (int i = 0; i < NT; ++i) TIE(gcs[i]);
          for (int spin = 0; spin < SPIN_CAP; ++spin) {
            bool bad = gst[1] != tag || gst[3] != tag || gfl != tag;
#pragma unroll
            for (int i = 0; i < NT; ++i) bad = bad || gcs[i][1] != tag;
            if (!any_set(bad)) break;
            __builtin_amdgcn_s_sleep(4);
            ldg();
            WAIT_V(0);
            TIE(gst); TIE(gfl);
#pragma unroll
            for (int i = 0; i < NT; ++i) TIE(gcs[i]);
          }
          sfor<0, 2>(ldaw);
        }
        if (s == 1) {
          rstd = asf(gst[2]);
          const float bop = hh ? __builtin_amdgcn_rcpf(rstd) : -asf(gst[0]);
#pragma unroll
          for (int i = 0; i < 3; ++i) acc[i] = __builtin_amdgcn_mfma_f32_32x32x2f32(asf(gcs[i][0]), bop, acc[i], 0, 0, 0);
          if (!TA) acc[3] = __builtin_amdgcn_mfma_f32_32x32x2f32(t < 16 ? asf(gcs[3][0]) : 0.f, bop, acc[3], 0, 0, 0);
          sfor<2, 4>(ldaw);
        }
        if (s == 2) sfor<4, NW5>(ldaw);
      }
      if constexpr (j == 6 && s == 0) sfor<NW5 + 6, NW5 + NW6>(ldaw);
    });
    if (j == 0) TS(3);
    if (j == 4) TS(4);
    if (j == 5) TS(5);
  });
  TS(6);
  constexpr int NWL = NW5 + NW6 + NW7;
  if (!TA) {
    *(LAS f32x4*)(size_t)(lds0 + L_T6 + w * 2048 + lo16) = (f32x4){acc[3][8], acc[3][9], acc[3][10], acc[3][11]};
    *(LAS f32x4*)(size_t)(lds0 + L_T6 + w * 2048 + 1024 + lo16) = (f32x4){acc[3][12], acc[3][13], acc[3][14], acc[3][15]};
    const float rs2 = rstd * rstd;
    const char* hfp = ws + WS_HF + (size_t)(tok32 * 112 + role * 14) * 1024;
    auto hpk = [&](float a, float c) { return pk2(fmaxf(a, 0.f) * (a * rs2), fmaxf(c, 0.f) * (c * rs2)); };
    sfor<0, 3>([&](auto J) {
      constexpr int j = decltype(J)::v;
      u32x4 o0, o1;
#pragma unroll
      for (int e = 0; e < 4; ++e) { o0[e] = hpk(acc[j][2 * e], acc[j][2 * e + 1]); o1[e] = hpk(acc[j][8 + 2 * e], acc[j][9 + 2 * e]); }
      const char* d = hfp + (size_t)(6 * hf + 2 * j) * 1024;
      st4<0>(d, lo16, o0); st4<1024>(d, lo16, o1);
    });
    WAIT_L0();
    BAR();
    {
      const f32x4 pa = *(LAS const f32x4*)(size_t)(lds0 + L_T6 + (w ^ 4) * 2048 + lo16);
      const f32x4 pb = *(LAS const f32x4*)(size_t)(lds0 + L_T6 + (w ^ 4) * 2048 + 1024 + lo16);
      u32x4 o;
      o[0] = hpk(acc[3][0] + pa[0], acc[3][1] + pa[1]); o[1] = hpk(acc[3][2] + pa[2], acc[3][3] + pa[3]);
      o[2] = hpk(acc[3][4] + pb[0], acc[3][5] + pb[1]); o[3] = hpk(acc[3][6] + pb[2], acc[3][7] + pb[3]);
      st4<0>(hfp + (size_t)(12 + hf) * 1024, lo16, o);
    }
  } else {
    const unsigned sgn = ((t >> 2) & 1) ? 0x80008000u : 0u;
    bf16x8 aphi[2];
#pragma unroll
    for (int e = 0; e < 2; ++e) {
      const unsigned a = lds0 + L_WPHI + (2 * hf + e) * 512 + (4 * hh + q) * 32 + (8 * blk + 4 * (p >> 1)) * 2;
      u32x4 u = __builtin_bit_cast(u32x4, tr_frag(a, a + 8 * 32));
      u[0] ^= sgn; u[1] ^= sgn; u[2] ^= sgn; u[3] ^= sgn;
      aphi[e] = __builtin_bit_cast(bf16x8, u);
    }
    const float rl2 = rstd * 1.4426950408889634f;
    const int tok = 32 * tg + t, xs = (tok >> 1) & 3;
#pragma unroll
    for (int e = 0; e < 2; ++e) {
      const bf16x8 kf = pack8(acc[1], 8 * e, rl2);
      const f32x16 pp = mfma32(aphi[e], kf, (f32x16){});
      u32x4 g0, g1; softmax16(pp, g0, g1);
      const unsigned r = lds0 + L_PK + (2 * hf + e) * 8192 + tok * 64;
      *(LAS u32x4*)(size_t)(r + (((2 * hh) ^ xs) << 4)) = g0;
      *(LAS u32x4*)(size_t)(r + (((2 * hh + 1) ^ xs) << 4)) = g1;
    }
    {
      const unsigned r = lds0 + L_VI + hf * 8192 + tok * 64 + 8 * hh;
#pragma unroll
      for (int g = 0; g < 4; ++g) {
        u32x2 o;
        o[0] = pk2(acc[2][4 * g] * rstd, acc[2][4 * g + 1] * rstd);
        o[1] = pk2(acc[2][4 * g + 2] * rstd, acc[2][4 * g + 3] * rstd);
        *(LAS u32x2*)(size_t)(r + ((g ^ xs) << 4)) = o;
      }
    }
    WAIT_L0();
    BAR();
    if (hf == 1) {
      f32x16 kv = (f32x16){};
#pragma unroll
      for (int u = 0; u < 8; ++u) {
        const int r0 = 16 * u + 4 * hh + q, r1 = r0 + 8;
        const int ca = 2 * blk + (p >> 1);
        const unsigned a0 = lds0 + L_PK + tg * 8192 + r0 * 64 + ((ca ^ ((r0 >> 1) & 3)) << 4) + 8 * (p & 1);
        const unsigned a1 = lds0 + L_PK + tg * 8192 + r1 * 64 + ((ca ^ ((r1 >> 1) & 3)) << 4) + 8 * (p & 1);
        const int cb = 2 * (tg & 1) + (p >> 1);
        const unsigned b0 = blk ? (lds0 + L_ONES) : (lds0 + L_VI + (tg >> 1) * 8192 + r0 * 64 + ((cb ^ ((r0 >> 1) & 3)) << 4) + 8 * (p & 1));
        const unsigned b1p = blk ? (lds0 + L_ONES) : (lds0 + L_VI + (tg >> 1) * 8192 + r1 * 64 + ((cb ^ ((r1 >> 1) & 3)) << 4) + 8 * (p & 1));
        kv = mfma32(tr_frag(a0, a1), tr_frag(b0, b1p), kv);
      }
      const char* kp = ws + WS_KVP + (size_t)((role * 4 + tg) * 8 * 16 + panel) * 1024;
      sfor<0, 8>([&](auto J) {
        constexpr int j = decltype(J)::v;
        st4_m(kp + (size_t)j * 16384, lo16, (u32x4){asu(kv[2 * j]), tag, asu(kv[2 * j + 1]), tag}, 0x0001ffff0001ffffull);
      });
    }
    const char* pq = ws + WS_PQ + (size_t)(tok32 * 32 + role * 4 + 2 * hf) * 2048;
#pragma unroll
    for (int e = 0; e < 2; ++e) {
      const bf16x8 qf = pack8(acc[0], 8 * e, rl2);
      const f32x16 pp = mfma32(aphi[e], qf, (f32x16){});
      u32x4 g0, g1; softmax16(pp, g0, g1);
      if (e == 0) { st4<0>(pq, lo16, g0); st4<1024>(pq, lo16, g1); } else { st4<2048>(pq, lo16, g0); st4<3072>(pq, lo16, g1); }
    }
  }
  TS(7);
  sfor<NWL, 28>(ldaw);
  WAIT_V(28 - NWL);
  BAR();
  if (w == 0) st1_m(ws + WS_FH + ((TA ? 0 : 128) + panel * 8 + role) * 128, 0, tag, 1ull);
  TS(8);
  bool kvflag = false;
  if (!TA && w < 2) {
    const int jid = (panel * 8 + role) * 2 + w, hd = jid >> 3, jj = jid & 7;
    const char* src = ws + WS_KVP + (size_t)(hd * 8 + jj) * 16384;
    const unsigned vo = hh * 1024 + blk * 512 + (l & 15) * 16;
    const unsigned vz = (l & 15) * 1024 + blk * 512 + 256;
    u32x4 pv[8], pz;
    for (int spin = 0; spin < SPIN_CAP; ++spin) {
      sfor<0, 8>([&](auto I) { constexpr int i = decltype(I)::v; ald4c<(i & 1) * 2048>(pv[i], src + (i >> 1) * 4096, vo); });
      ald4c<0>(pz, src, vz);
      WAIT_V(0);
      bool bad = false;
#pragma unroll
      for (int i = 0; i < 8; ++i) { TIE(pv[i]); bad = bad || pv[i][1] != tag || pv[i][3] != tag; }
      TIE(pz); bad = bad || pz[1] != tag || pz[3] != tag;
      if (!any_set(bad)) break;
      __builtin_amdgcn_s_sleep(4);
    }
    float s0 = 0.f, s1 = 0.f;
#pragma unroll
    for (int i = 0; i < 8; ++i) { s0 += asf(pv[i][0]); s1 += asf(pv[i][2]); }
    s0 = sw32_add(s0, s0); s1 = sw32_add(s1, s1);
    float z0 = asf(pz[0]), z1 = asf(pz[2]);
    z0 = dpp_add<0x128>(z0); z0 = dpp_add<0x124>(z0); z0 = dpp_add<0x122>(z0); z0 = dpp_add<0x121>(z0);
    z1 = dpp_add<0x128>(z1); z1 = dpp_add<0x124>(z1); z1 = dpp_add<0x122>(z1); z1 = dpp_add<0x121>(z1);
    const unsigned val = hh ? pk2(z0, z1) : pk2(s0, s1);
    const unsigned c = hh ? 16 : (l & 15);
    st1_m(ws + WS_KVZ + (size_t)(hd * 4 + (jj >> 1)) * 272, c * 16 + (2 * blk + (jj & 1)) * 4, val, 0x00010001ffffffffull);
    kvflag = true;
  }
  {
    const char* fa = ws + WS_FH + (128 + (tt >> 1) * 8 + w) * 128;
    for (int spin = 0; spin < SPIN_CAP; ++spin) {
      unsigned f;
      ald1c<0>(f, fa, 0);
      WAIT_V(0);
      TIE(f);
      if (kvflag) { const int jid = (panel * 8 + role) * 2 + w; st1_m(ws + WS_FK, jid * 4, tag, 1ull); kvflag = false; }
      if (__builtin_amdgcn_readfirstlane(f) == tag) break;
      __builtin_amdgcn_s_sleep(2);
    }
  }
#pragma unroll
  for (int i = 0; i < 28; ++i) TIE(AW[i]);
  TS(9);
  {
  int l2;
  asm volatile("v_mbcnt_lo_u32_b32 %0, -1, 0\n\tv_mbcnt_hi_u32_b32 %0, -1, %0" : "=v"(l2));
  const int l = l2, t = l2 & 31, hh = l2 >> 5, tid = w * 64 + l2;
  const unsigned lo16 = l2 * 16;
  (void)l;
  const __amdgpu_buffer_rsrc_t rs = __builtin_amdgcn_make_buffer_rsrc((void*)ws, 0, (int)WS_END, 0x00020000);
  const unsigned hp = WS_HF + ((2 * tt) * 112 + 14 * w) * 1024;
  const unsigned wto = WS_WT + (ct * 144 + 112 + 4 * w) * 2048;
  const unsigned pqo = WS_PQ + ((2 * tt) * 32 + 4 * w) * 2048;
  auto ldc = [&](unsigned so) { return __builtin_amdgcn_raw_buffer_load_b128(rs, lo16, so, 16); };
  u32x4 B0[14], B1[14], WA[4][2], PQ[4][2][2], KZ[4][2];
  auto ldh = [&](int n) { B0[n] = ldc(hp + n * 1024); B1[n] = ldc(hp + (112 + n) * 1024); };
  auto ldq = [&](int hd, int j) { PQ[hd][j][0] = ldc(pqo + (32 * j + hd) * 2048); PQ[hd][j][1] = ldc(pqo + (32 * j + hd) * 2048 + 1024); };
#pragma unroll
  for (int n = 0; n < P2_D; ++n) ldh(n);
  SCHED();
  f32x16 ac[2][2];
#pragma unroll
  for (int i = 0; i < 2; ++i)
#pragma unroll
    for (int j = 0; j < 2; ++j) ac[i][j] = (f32x16){};
  gu32* fpq = (gu32*)(ws + WS_FH + ((tt >> 1) * 8 + w) * 128);
  gu32* fkz = (gu32*)(ws + WS_FK + w * 128 + t * 4);
  unsigned flq = 0, flk = 0;
  const int tokr = tid >> 3, n0 = (tid & 7) * 8;
  const size_t go = (size_t)(tt * 64 + tokr) * 512 + ct * 64 + n0;
  u32x4 xa, ba;
  static_assert(P2_D == 7, "schedule below assumes a window of 7");
#pragma unroll
  for (int n = 0; n < 14; ++n) {
    if (n + P2_D < 14) ldh(n + P2_D);
    if (n == 2) flq = __hip_atomic_load(fpq, RLX_AGENT);
    if (n == 6) {
      for (int spin = 0; spin < SPIN_CAP && flq != tag; ++spin) { __builtin_amdgcn_s_sleep(2); flq = __hip_atomic_load(fpq, RLX_AGENT); }
    }
    if (n >= 7 && n < 11) { ldq(n - 7, 0); WA[n - 7][0] = ldc(wto + (n - 7) * 2048); WA[n - 7][1] = ldc(wto + (n - 7) * 2048 + 1024); }
    if (n == 8) flk = __hip_atomic_load(fkz, RLX_AGENT);
    if (n == 11) {
      for (int spin = 0; spin < SPIN_CAP && any_set(flk != tag); ++spin) { __builtin_amdgcn_s_sleep(2); flk = __hip_atomic_load(fkz, RLX_AGENT); }
      const unsigned kzo = (t < 16 ? t : 16) * 16 + hh * 2 * 272;
#pragma unroll
      for (int hd = 0; hd < 4; ++hd)
#pragma unroll
        for (int e = 0; e < 2; ++e) KZ[hd][e] = __builtin_amdgcn_raw_buffer_load_b128(rs, kzo, WS_KVZ + (4 * w + hd) * 1088 + e * 272, 16);
    }
    if (n >= 12) { ldq(2 * (n - 12), 1); ldq(2 * (n - 12) + 1, 1); }
    SCHED();
    ac[0][0] = mfma32u(AW[2 * n], B0[n], ac[0][0]);
    ac[0][1] = mfma32u(AW[2 * n], B1[n], ac[0][1]);
    ac[1][0] = mfma32u(AW[2 * n + 1], B0[n], ac[1][0]);
    ac[1][1] = mfma32u(AW[2 * n + 1], B1[n], ac[1][1]);
    SCHED();
    if (n == 0) TS(10);
    if (n == 6) TS(11);
  }
  TS(12);
  xa = *(const u32x4*)(x + go); ba = *(const u32x4*)(b2 + ct * 64 + n0);
#pragma unroll
  for (int j = 0; j < 2; ++j) {
#pragma unroll
    for (int hd = 0; hd < 4; ++hd) {
      f32x16 nm = mfma32u(KZ[hd][0], PQ[hd][j][0], (f32x16){});
      nm = mfma32u(KZ[hd][1], PQ[hd][j][1], nm);
      const float rd = __builtin_amdgcn_rcpf(nm[8] + 1e-6f);
      const bf16x8 of = pack8(nm, 0, rd);
      ac[0][j] = mfma32(__builtin_bit_cast(bf16x8, WA[hd][0]), of, ac[0][j]);
      ac[1][j] = mfma32(__builtin_bit_cast(bf16x8, WA[hd][1]), of, ac[1][j]);
    }
#pragma unroll
    for (int g = 0; g < 4; ++g) {
      const unsigned a = lds0 + w * (64 * P2_RS) + (32 * j + t) * P2_RS + (16 * g + 8 * hh) * 4;
      *(LAS f32x4*)(size_t)a = (f32x4){ac[0][j][4 * g], ac[1][j][4 * g], ac[0][j][4 * g + 1], ac[1][j][4 * g + 1]};
      *(LAS f32x4*)(size_t)(a + 16) = (f32x4){ac[0][j][4 * g + 2], ac[1][j][4 * g + 2], ac[0][j][4 * g + 3], ac[1][j][4 * g + 3]};
    }
  }
  TS(13);
  WAIT_L0();
  BAR();
  {
    const unsigned ra = lds0 + tokr * P2_RS + n0 * 4;
    f32x4 s0 = *(LAS const f32x4*)(size_t)ra, s1 = *(LAS const f32x4*)(size_t)(ra + 16);
#pragma unroll
    for (int ww = 1; ww < 8; ++ww) {
      s0 += *(LAS const f32x4*)(size_t)(ra + ww * (64 * P2_RS));
      s1 += *(LAS const f32x4*)(size_t)(ra + ww * (64 * P2_RS) + 16);
    }
    u32x4 o;
    o[0] = pk2(s0[0] + blo(xa[0]) + blo(ba[0]), s0[1] + bhi(xa[0]) + bhi(ba[0]));
    o[1] = pk2(s0[2] + blo(xa[1]) + blo(ba[1]), s0[3] + bhi(xa[1]) + bhi(ba[1]));
    o[2] = pk2(s1[0] + blo(xa[2]) + blo(ba[2]), s1[1] + bhi(xa[2]) + bhi(ba[2]));
    o[3] = pk2(s1[2] + blo(xa[3]) + blo(ba[3]), s1[3] + bhi(xa[3]) + bhi(ba[3]));
    *(u32x4*)(out + go) = o;
  }
  }
  if (b == 0 && w == 0) st1_m(ws + WS_E, 0, tag, 1ull);
  TS(14);
  TS(15);
  TS_OUT();
}

__global__ __launch_bounds__(512, 2) void k_fused(const u16* __restrict__ x, const u16* __restrict__ wqkv, const u16* __restrict__ wphi,
                                                  const u16* __restrict__ wo, const u16* __restrict__ w1, const u16* __restrict__ b1,
                                                  const u16* __restrict__ w2, const u16* __restrict__ b2, char* __restrict__ ws, u16* __restrict__ out) {
  extern __shared__ __attribute__((aligned(16))) char smem[];
  const unsigned lds0 = (unsigned)(size_t)(LAS char*)smem;
  const int b = blockIdx.x;
  if ((b >> 6) < 2) body<true>(lds0, b, x, wqkv, wphi, wo, w1, b1, w2, b2, ws, out);
  else body<false>(lds0, b, x, wqkv, wphi, wo, w1, b1, w2, b2, ws, out);
}

extern "C" void kernel_launch(void* const* d_in, const int* in_sizes, int n_in, void* d_out, int out_size, void* d_ws, size_t ws_size, hipStream_t stream) {
  const u16* x = (const u16*)d_in[0];
  const u16* wqkv = (const u16*)d_in[1];
  const u16* wphi = (const u16*)d_in[2];
  const u16* wo = (const u16*)d_in[3];
  const u16* w1 = (const u16*)d_in[5];
  const u16* b1 = (const u16*)d_in[6];
  const u16* w2 = (const u16*)d_in[7];
  const u16* b2 = (const u16*)d_in[8];
  hipFuncSetAttribute((const void*)k_fused, hipFuncAttributeMaxDynamicSharedMemorySize, LDS_BYTES);
  k_fused<<<256, 512, LDS_BYTES, stream>>>(x, wqkv, wphi, wo, w1, b1, w2, b2, (char*)d_ws, (u16*)d_out);
}
```

```cpp
#define JSL 1
#define LDGJ 6
#define OST 4
#define EARLYK 1
#define NOT1 1
#define AWA 0,0,0,0,2,2,2,2
#define AWB 0,0,0,0,2,2,2,2
#include <hip/hip_runtime.h>
#include <hip/hip_bf16.h>
#include <stdint.h>

#define LAS __attribute__((address_space(3)))
#define GAS __attribute__((address_space(1)))
typedef __bf16 bf16x8 __attribute__((ext_vector_type(8)));
typedef __bf16 bf16x2 __attribute__((ext_vector_type(2)));
typedef short s16x4 __attribute__((ext_vector_type(4)));
typedef short s16x8 __attribute__((ext_vector_type(8)));
typedef float f32x16 __attribute__((ext_vector_type(16)));
typedef float f32x4 __attribute__((ext_vector_type(4)));
typedef float f32x2 __attribute__((ext_vector_type(2)));
typedef unsigned u32x4 __attribute__((ext_vector_type(4)));
typedef unsigned u32x2 __attribute__((ext_vector_type(2)));
typedef unsigned short u16;
typedef GAS unsigned gu32;
#define RLX_AGENT __ATOMIC_RELAXED, __HIP_MEMORY_SCOPE_AGENT

constexpr unsigned WS_E = 0;
constexpr unsigned WS_FH = 1024;
constexpr unsigned WS_ST = WS_FH + 32768;
constexpr unsigned WS_CS = WS_ST + 32768;
constexpr unsigned WS_KVZ = WS_CS + 53248;
constexpr unsigned WS_KVP = WS_KVZ + 69632;
constexpr unsigned WS_HF = WS_KVP + (4u << 20);
constexpr unsigned WS_PQ = WS_HF + 64 * 112 * 1024;
constexpr unsigned WS_END = WS_PQ + 64 * 32 * 1024;

template <int N> struct IC { static constexpr int v = N; };
template <int I, int N, class F> __device__ __forceinline__ void sfor(F&& f) { if constexpr (I < N) { f(IC<I>{}); sfor<I + 1, N>(f); } }

__device__ __forceinline__ void glds16(const void* sbase, unsigned voff, unsigned lds_dst) {
  asm volatile("s_mov_b32 m0, %2\n\ts_nop 0\n\tglobal_load_lds_dwordx4 %0, %1" ::"v"(voff), "s"(sbase), "s"(lds_dst) : "memory");
}
__device__ __forceinline__ void glds16_lo(const void* sbase, unsigned voff, unsigned lds_dst) {
  asm volatile("s_mov_b32 m0, %2\n\ts_mov_b32 exec_hi, 0\n\tglobal_load_lds_dwordx4 %0, %1\n\ts_mov_b32 exec_hi, -1" ::"v"(voff), "s"(sbase), "s"(lds_dst) : "memory");
}
template <int OFF> __device__ __forceinline__ void ald4(u32x4& d, const void* sb, unsigned vo) {
  asm volatile("global_load_dwordx4 %0, %1, %2 offset:%3 ; ALD" : "=v"(d) : "v"(vo), "s"(sb), "i"(OFF) : "memory");
}
template <int OFF> __device__ __forceinline__ void ald4c(u32x4& d, const void* sb, unsigned vo) {
  asm volatile("global_load_dwordx4 %0, %1, %2 offset:%3 sc1 ; ALD" : "=v"(d) : "v"(vo), "s"(sb), "i"(OFF) : "memory");
}
template <int OFF> __device__ __forceinline__ void ald2c(u32x2& d, const void* sb, unsigned vo) {
  asm volatile("global_load_dwordx2 %0, %1, %2 offset:%3 sc1 ; ALD" : "=v"(d) : "v"(vo), "s"(sb), "i"(OFF) : "memory");
}
template <int OFF> __device__ __forceinline__ void ald1c(unsigned& d, const void* sb, unsigned vo) {
  asm volatile("global_load_dword %0, %1, %2 offset:%3 sc1 ; ALD" : "=v"(d) : "v"(vo), "s"(sb), "i"(OFF) : "memory");
}
template <int OFF> __device__ __forceinline__ void ald1(unsigned& d, const void* sb, unsigned vo) {
  asm volatile("global_load_dword %0, %1, %2 offset:%3 ; ALD" : "=v"(d) : "v"(vo), "s"(sb), "i"(OFF) : "memory");
}
template <int OFF> __device__ __forceinline__ void aldkz(u32x4& a, u32x4& c, const void* sb, unsigned vo) {
  asm volatile("s_mov_b64 exec, %4\n\tglobal_load_dwordx4 %0, %2, %3 offset:%5 sc1 ; ALD\n\tglobal_load_dwordx4 %1, %2, %3 offset:%6 sc1 ; ALD\n\ts_mov_b64 exec, -1"
               : "=&v"(a), "=&v"(c) : "v"(vo), "s"(sb), "s"(0x0011ffff0011ffffull), "i"(OFF), "i"(OFF + 16) : "memory");
}
__device__ __forceinline__ void atouch(unsigned& d, const void* sb, unsigned vo) {
  asm volatile("s_mov_b64 exec, 0xff\n\tglobal_load_dword %0, %1, %2 ; TCH\n\ts_mov_b64 exec, -1" : "+v"(d) : "v"(vo), "s"(sb) : "memory");
}
__device__ __forceinline__ void aldh(unsigned& d, const void* sb, unsigned vo) {
  asm volatile("global_load_ushort %0, %1, %2 ; ALD" : "=v"(d) : "v"(vo), "s"(sb) : "memory");
}
#define TIE(x) asm volatile("; TIE %0" : "+v"(x))
__device__ __forceinline__ void tie4(u32x4& x) { TIE(x); }
__device__ __forceinline__ void tie1(unsigned& x) { TIE(x); }
template <int OFF> __device__ __forceinline__ void st4(const void* sb, unsigned vo, u32x4 v) {
  asm volatile("global_store_dwordx4 %0, %1, %2 offset:%3 sc1\n\ts_nop 1" ::"v"(vo), "v"(v), "s"(sb), "i"(OFF) : "memory");
}
__device__ __forceinline__ void st1(const void* sb, unsigned vo, unsigned v) {
  asm volatile("global_store_dword %0, %1, %2 sc1" ::"v"(vo), "v"(v), "s"(sb) : "memory");
}
__device__ __forceinline__ void st4_m(const void* sb, unsigned vo, u32x4 v, unsigned long long mask) {
  asm volatile("s_mov_b64 exec, %3\n\tglobal_store_dwordx4 %0, %1, %2 sc1\n\ts_mov_b64 exec, -1\n\ts_nop 0" ::"v"(vo), "v"(v), "s"(sb), "s"(mask) : "memory");
}
__device__ __forceinline__ void st2_m(const void* sb, unsigned vo, u32x2 v, unsigned long long mask) {
  asm volatile("s_mov_b64 exec, %3\n\tglobal_store_dwordx2 %0, %1, %2 sc1\n\ts_mov_b64 exec, -1\n\ts_nop 0" ::"v"(vo), "v"(v), "s"(sb), "s"(mask) : "memory");
}
__device__ __forceinline__ void st1_m(const void* sb, unsigned vo, unsigned v, unsigned long long mask) {
  asm volatile("s_mov_b64 exec, %3\n\tglobal_store_dword %0, %1, %2 sc1\n\ts_mov_b64 exec, -1" ::"v"(vo), "v"(v), "s"(sb), "s"(mask) : "memory");
}
#define WAIT_V(n) asm volatile("s_waitcnt vmcnt(%0)" ::"i"(n) : "memory")
#define WAIT_L0() asm volatile("s_waitcnt lgkmcnt(0)" ::: "memory")
#define BAR() do { asm volatile("" ::: "memory"); __builtin_amdgcn_s_barrier(); asm volatile("" ::: "memory"); } while (0)
#define SCHED() __builtin_amdgcn_sched_barrier(0)
#define TS_DECL
#define TS(i)
#define TS_OUT()

__device__ __forceinline__ bf16x8 tr_frag(unsigned a0, unsigned a1) {
  s16x4 a = __builtin_amdgcn_ds_read_tr16_b64_v4i16((LAS s16x4*)(size_t)a0);
  s16x4 b = __builtin_amdgcn_ds_read_tr16_b64_v4i16((LAS s16x4*)(size_t)a1);
  s16x8 c = __builtin_shufflevector(a, b, 0, 1, 2, 3, 4, 5, 6, 7);
  return __builtin_bit_cast(bf16x8, c);
}
__device__ __forceinline__ f32x16 mfma32(bf16x8 a, bf16x8 b, f32x16 c) { return __builtin_amdgcn_mfma_f32_32x32x16_bf16(a, b, c, 0, 0, 0); }
__device__ __forceinline__ f32x16 mfma32u(u32x4 a, u32x4 b, f32x16 c) { return mfma32(__builtin_bit_cast(bf16x8, a), __builtin_bit_cast(bf16x8, b), c); }
__device__ __forceinline__ unsigned pk2(float a, float b) {
  bf16x2 v = {(__bf16)a, (__bf16)b};
  return __builtin_bit_cast(unsigned, v);
}
__device__ __forceinline__ bf16x8 pack8(const f32x16& v, int base, float s) {
  u32x4 r;
  const f32x2 s2 = {s, s};
#pragma unroll
  for (int i = 0; i < 4; ++i) { const f32x2 a = (f32x2){v[base + 2 * i], v[base + 2 * i + 1]} * s2; r[i] = pk2(a[0], a[1]); }
  return __builtin_bit_cast(bf16x8, r);
}
__device__ __forceinline__ float asf(unsigned u) { return __builtin_bit_cast(float, u); }
__device__ __forceinline__ unsigned asu(float f) { return __builtin_bit_cast(unsigned, f); }
__device__ __forceinline__ float blo(unsigned u) { return asf(u << 16); }
__device__ __forceinline__ float bhi(unsigned u) { return asf(u & 0xffff0000u); }
template <int CTRL> __device__ __forceinline__ float dpp_add(float v) { return v + asf((unsigned)__builtin_amdgcn_update_dpp(0, (int)asu(v), CTRL, 0xf, 0xf, false)); }
__device__ __forceinline__ float sw16_add(float a, float b) {
  auto r = __builtin_amdgcn_permlane16_swap(asu(a), asu(b), false, false); return asf(r[0]) + asf(r[1]);
}
__device__ __forceinline__ float sw32_add(float a, float b) {
  auto r = __builtin_amdgcn_permlane32_swap(asu(a), asu(b), false, false); return asf(r[0]) + asf(r[1]);
}
__device__ __forceinline__ float wave_sum(float v) {
  v = dpp_add<0x128>(v); v = dpp_add<0x124>(v); v = dpp_add<0x122>(v); v = dpp_add<0x121>(v);
  v = sw16_add(v, v); v = sw32_add(v, v);
  return v;
}
__device__ __forceinline__ bool any_set(bool c) { return __builtin_amdgcn_ballot_w64(c) != 0ull; }

__device__ __forceinline__ unsigned pk2v(f32x2 v) { return pk2(v[0], v[1]); }
__device__ __forceinline__ void softmax16(const f32x16& p, float s, u32x4& f0, u32x4& f1) {
  float mx = fmaxf(fmaxf(p[0], p[1]), p[2]);
#pragma unroll
  for (int i = 3; i < 15; i += 2) mx = fmaxf(fmaxf(mx, p[i]), p[i + 1]);
  mx = fmaxf(mx, p[15]);
  const float nm = -mx * s;
  const f32x2 s2 = {s, s}, n2 = {nm, nm};
  f32x2 e[8];
#pragma unroll
  for (int i = 0; i < 8; ++i) {
    const f32x2 a = __builtin_elementwise_fma((f32x2){p[2 * i], p[2 * i + 1]}, s2, n2);
    e[i] = (f32x2){__builtin_amdgcn_exp2f(a[0]), __builtin_amdgcn_exp2f(a[1])};
  }
  const f32x2 t = ((e[0] + e[1]) + (e[2] + e[3])) + ((e[4] + e[5]) + (e[6] + e[7]));
  const float r = __builtin_amdgcn_rcpf(t[0] + t[1]);
  const f32x2 r2 = {r, r};
#pragma unroll
  for (int i = 0; i < 4; ++i) { f0[i] = pk2v(e[i] * r2); f1[i] = pk2v(e[4 + i] * r2); }
}
__device__ __forceinline__ bf16x8 pack8n(const f32x16& v, int base) {
  u32x4 r;
  r[0] = pk2(v[base + 0], v[base + 1]); r[1] = pk2(v[base + 2], v[base + 3]);
  r[2] = pk2(v[base + 4], v[base + 5]); r[3] = pk2(v[base + 6], v[base + 7]);
  return __builtin_bit_cast(bf16x8, r);
}

constexpr int L_XS = 16384;
constexpr int NXS = 4;
constexpr int L_W = NXS * L_XS;
constexpr int L_CSX = 151552;
constexpr int L_WPHI = L_CSX + 1024;
constexpr int L_ONES = 139264;
constexpr int L_PK = 16384;
constexpr int L_VI = 0;
constexpr int L_T6 = 0;
constexpr int P2_RS = 272;
constexpr int P2_WS = 64 * P2_RS;
constexpr int LDS_BYTES = 8 * P2_WS + 16384;
constexpr int SPIN_CAP = 20000;
constexpr int aw_cnt(bool ta, int j) { constexpr int a[8] = {AWA}, b[8] = {AWB}; return j < 0 || j > 7 ? 0 : ta ? a[j] : b[j]; }
constexpr int aw_base(bool ta, int j) { int s = 0; for (int i = 0; i < j; ++i) s += aw_cnt(ta, i); return s; }
#define PB 0
#define P2_D 7
#define NPRE 12
#define APRE 0
#define AEP 0, 0, 0, 0, 0
#define JDUP 0
#define AJOB 0
#define KSL 4
#define HSL 2
#define OSL 2
#define TCH 1, 1, 1, 1, 1, 1, 1, 2
constexpr int tc_cnt(int j) { constexpr int a[8] = {TCH}; return j < 0 || j > 7 ? 0 : a[j]; }
constexpr int tc_base(int j) { int s = 0; for (int i = 0; i < j; ++i) s += tc_cnt(i); return s; }
#define OFN 6

constexpr int ae_cnt(int p) { constexpr int a[5] = {AEP}; return p < 0 || p > 4 ? 0 : a[p]; }
constexpr int ae_base(int p) { int s = 0; for (int i = 0; i < p; ++i) s += ae_cnt(i); return s; }

struct Fr { bf16x8 x, w0, w1, w2, w6; };

template <bool TA>
__device__ __forceinline__ void body(unsigned lds0, int b, const u16* __restrict__ x, const u16* __restrict__ wqkv, const u16* __restrict__ wphi,
                                     const u16* __restrict__ wo, const u16* __restrict__ w1, const u16* __restrict__ b1, const u16* __restrict__ w2,
                                     const u16* __restrict__ b2, char* __restrict__ ws, u16* __restrict__ out) {
  constexpr int ROWB = TA ? 384 : 448;
  constexpr int NCH = ROWB / 16;
  constexpr int NPW = 3;
  constexpr int NP = NPW + 2;
  constexpr int L_WS = 64 * ROWB;
  constexpr int WROW = TA ? 3072 : 3584;
  constexpr int NT = TA ? 3 : 4;
  constexpr int G = TA ? NT + 1 : 2 * NT + 1;
  TS_DECL; TS(0);
  const int xcd = b & 7, idx = b >> 3;
  const int panel = (xcd & 1) * 8 + (idx & 7), sel = idx >> 3, role = (xcd >> 1) + 4 * (sel & 1);
  const int tt = 4 * xcd + sel, ct = idx & 7;
  int tid = threadIdx.x;
  asm volatile("; body %1" : "+v"(tid) : "i"((int)TA));
  const int l = tid & 63, w = __builtin_amdgcn_readfirstlane(tid >> 6);
  const int tg = w & 3, hf = w >> 2;
  const int t = l & 31, hh = l >> 5;
  unsigned E;
  unsigned woff[4];
#pragma unroll
  for (int i = 0; i < 4; ++i) {
    const int n = 64 * i + l, row = n / NCH, c = n % NCH;
    if (TA) { const int cl = c ^ (((row >> 1) & 1) << 2); woff[i] = row * WROW + (cl >> 3) * 1024 + role * 128 + (cl & 7) * 16; }
    else { const int o = (hf ? 16 + 3 * tg + i : 4 * tg + i) * 64 + l, r2 = o / 28, c2 = o % 28; woff[i] = r2 * WROW + role * 448 + c2 * 16; }
  }
  unsigned xoff[2];
#pragma unroll
  for (int i = 0; i < 2; ++i) { const int row = 8 * i + (l >> 3), c = (l & 7) ^ ((row >> 1) & 7); xoff[i] = row * 1024 + c * 16; }
  const char* wmat = (const char*)(TA ? wqkv : w1);
  const char* wbase = TA ? wmat + (size_t)(8 * w) * WROW : wmat;
  const char* xbase = (const char*)x + (size_t)(panel * 128 + 16 * w) * 1024;
  const unsigned wdst = lds0 + L_W + (TA ? w * 8 * ROWB : (hf ? 16 + 3 * tg : 4 * tg) * 1024), xdst = lds0 + w * 2048;
  auto gw = [&](int j, int piece) {
    const char* wb = wbase + (size_t)j * 64 * WROW;
    const unsigned d = wdst + (j % 3) * L_WS + piece * 1024;
    if (piece < 3) glds16(wb, woff[piece], d); else if (hf == 0) glds16(wb, woff[3], d);
  };
  auto gx = [&](int j, int piece) { glds16(xbase + j * 128, xoff[piece], xdst + (j % NXS) * L_XS + piece * 1024); };
  auto issue0 = [&](int j) { gw(j, 0); gw(j, 1); gw(j, 2); };
  auto issue1 = [&](int j) { if (!TA) gw(j, 3); gx(j, 0); gx(j, 1); };
  asm volatile("" ::"v"(woff[0]), "v"(woff[1]), "v"(woff[2]), "v"(xoff[0]), "v"(xoff[1]));
  asm volatile("" ::"s"(x), "s"(wqkv), "s"(wphi), "s"(wo), "s"(w1), "s"(b1), "s"(w2), "s"(b2), "s"(ws), "s"(out));
  issue0(0); issue1(0);
  issue0(1); issue1(1);
  issue0(2); issue1(2);
  gx(3, 0); gx(3, 1);
  asm volatile("s_load_dword %0, %1, 0x0" : "=s"(E) : "s"(ws) : "memory");
  u32x4 jx, jc0, jc1;
  const int stok = panel * 128 + ((TA ? 0 : 8) + role) * 8 + w;
  constexpr int NCJ = TA ? 12 : 14;
  const int cp = panel < NCJ ? panel : panel - NCJ;
  const int ccol = TA ? (cp >> 2) * 512 + role * 64 + ((cp >> 1) & 1) * 32 : role * 224 + (cp >> 1) * 32;
  ald4<0>(jx, (const char*)x + (size_t)stok * 1024, l * 16);
  {
    const unsigned vo = ((cp & 1) * 256 + 32 * w + (l >> 2)) * WROW + ccol * 2 + (l & 3) * 16;
    ald4<0>(jc0, wmat, vo); ald4<0>(jc1, wmat + 16 * WROW, vo);
  }
  unsigned jt;
  {
    const char* ta;
    ta = (const char*)b2;
    if (w == 4) ta = l < 16 ? ws + (size_t)l * 1048576u : l < 18 ? (const char*)out + (size_t)(l - 16) * 1048576u : l < 20 ? (const char*)w2 + (size_t)(l - 18) * 1048576u : (const char*)wo;
    asm volatile("global_load_dword %0, %1, off ; ALD" : "=v"(jt) : "v"(ta) : "memory");
  }
  constexpr int JP = 4;
  if (TA) *(LAS u32x4*)(size_t)(lds0 + L_ONES + tid * 16) = (u32x4){0x3F803F80u, 0x3F803F80u, 0x3F803F80u, 0x3F803F80u};
  const int blk = (l >> 4) & 1, q = (l & 15) >> 2, p = l & 3;
  const int sw = TA ? ((q >> 1) & 1) : 0;
  const unsigned wlane = lds0 + L_W + (8 * hh + q) * ROWB + 8 * p + (TA ? (32 * blk + 64 * (hf ^ sw)) : (32 * blk + 192 * hf));
  const unsigned w6lane = lds0 + L_W + (8 * hh + q) * ROWB + 8 * p + 32 * (blk ^ hf) + 384;
  unsigned wad[4], xad[4], w6ad[2];
#pragma unroll
  for (int i = 0; i < 4; ++i) {
    const int ks = (2 * hf + i) & 3;
    wad[i] = wlane + ks * 16 * ROWB;
    xad[i] = lds0 + tg * 4096 + t * 128 + (((2 * ks + hh) ^ ((t >> 1) & 7)) << 4);
    if (i < 2) w6ad[i] = w6lane + ks * 16 * ROWB;
  }
  constexpr int TSB = TA ? 128 : 64;
  auto rdx = [&](Fr& f, int i, int slot) { f.x = *(LAS const bf16x8*)(size_t)(xad[i] + slot * L_XS); };
  auto rdw = [&](int i, int slot, int j) { const unsigned a = wad[i] + slot * L_WS + TSB * j; return tr_frag(a, a + 4 * ROWB); };
  auto rd6 = [&](int i, int slot) { const unsigned a = w6ad[i] + slot * L_WS; return tr_frag(a, a + 4 * ROWB); };
  f32x16 acc[4];
#pragma unroll
  for (int i = 0; i < 4; ++i) acc[i] = (f32x16){};
  u32x4 AU[28];
  const unsigned uoff = (l >> 3) * 1024 + (((l & 7) ^ (((l >> 4) & 1) << 2)) << 4);
  const char* awb = (const char*)w2 + (size_t)(28 * w) * 8192 + ct * 128;
  const unsigned lo16 = l * 16;
  auto ldau = [&](auto U) { constexpr int u = decltype(U)::v; ald4<0>(AU[u], awb + (size_t)u * 8192, uoff); };
  unsigned jw = 0;
  const char* tw2 = (const char*)w2 + (size_t)(224 * (idx >> 2) + 8 * (idx & 3) + w) * 1024;
  const char* two = (const char*)wo + (size_t)(16 * idx + w) * 1024;
  auto touch = [&](int r) { atouch(jw, r < 7 ? tw2 + r * 32768 : two + (r - 7) * 8192, l * 128); };
  u32x4 gst; u32x2 gcs[4]; unsigned gb[4];
  const int tok32 = panel * 4 + tg;
  auto ldg = [&]() {
    ald4c<0>(gst, ws + WS_ST, (tok32 * 32 + t) * 16);
#pragma unroll
    for (int j = 0; j < NT; ++j) {
      const int col = TA ? j * 512 + role * 64 + hf * 32 + t : 1536 + role * 224 + (j < 3 ? 32 * (3 * hf + j) + t : 192 + (t ^ (16 * hf)));
      ald2c<0>(gcs[j], ws + WS_CS, col * 16 + hh * 8);
    }
  };
  auto ldb = [&]() {
#pragma unroll
    for (int j = 0; j < NT; ++j) {
      const int col = role * 224 + (j < 3 ? 32 * (3 * hf + j) + t : 192 + (t ^ (16 * hf)));
      aldh(gb[j], b1, col * 2);
    }
  };
  unsigned tag;
  float rstd;
  Fr f0, f1;
  TS(1);
  WAIT_V(2 * NP + 2 + JP);
  asm volatile("s_waitcnt lgkmcnt(0)" : "+s"(E)::"memory");
  tag = E + 1;
  BAR();
  TS(2);
  rdx(f0, 0, 0); f0.w0 = rdw(0, 0, 0); f0.w1 = rdw(0, 0, 1); f0.w2 = rdw(0, 0, 2);
  if (!TA) f0.w6 = rd6(0, 0);

  auto step = [&](Fr& fc, Fr& fn, auto I, auto NI, auto NJ, auto RD, auto&& fill) {
    constexpr int i = decltype(I)::v, ni = decltype(NI)::v, nsl = decltype(NJ)::v % 3, nxs = decltype(NJ)::v % NXS;
    constexpr bool rd = decltype(RD)::v != 0;
    acc[0] = mfma32(fc.w0, fc.x, acc[0]);
    SCHED();
    if (rd) { rdx(fn, ni, nxs); fn.w0 = rdw(ni, nsl, 0); }
    fill(IC<0>{});
    SCHED();
    acc[1] = mfma32(fc.w1, fc.x, acc[1]);
    SCHED();
    if (rd) { fn.w1 = rdw(ni, nsl, 1); fn.w2 = rdw(ni, nsl, 2); }
    fill(IC<1>{});
    SCHED();
    acc[2] = mfma32(fc.w2, fc.x, acc[2]);
    SCHED();
    if (!TA && rd && ni < 2) fn.w6 = rd6(ni, nsl);
    fill(IC<2>{});
    SCHED();
    if (!TA && i < 2) { acc[3] = mfma32(fc.w6, fc.x, acc[3]); SCHED(); }
  };
  constexpr int NPL = NP;
  constexpr auto ops = [](int j, int st) {
    const int x1 = j >= 1 && j + 3 < 8 ? 1 : 0;
    return st == 0 ? aw_cnt(TA, j) / 2 + (j == LDGJ ? G : 0) + (j >= 1 && j + 2 < 8 ? 1 : 0) : st == 1 ? tc_cnt(j) + x1 : st == 2 ? aw_cnt(TA, j) / 2 + x1 : (j + 3 < 8 ? 2 : 0) + (j == 1 ? 1 : 0);
  };
  constexpr auto ops_from = [ops](int j0, int st0, int j1, int st1) {
    int n = 0;
    for (int k = 4 * j0 + st0; k <= 4 * j1 + st1; ++k) n += ops(k >> 2, k & 3);
    return n;
  };
  auto awfill = [&](auto JJ, auto ST, auto S) {
    constexpr int j = decltype(JJ)::v, st = decltype(ST)::v, s = decltype(S)::v;
    constexpr int per = aw_cnt(TA, j) / 2;
    if constexpr (s < per) ldau(IC<aw_base(TA, j) + st * per + s>{});
  };
  sfor<0, 8>([&](auto JJ) {
    constexpr int j = decltype(JJ)::v;
    constexpr int sl = j, nsl = j + 1;
    step(f0, f1, IC<0>{}, IC<1>{}, IC<sl>{}, IC<1>{}, [&](auto S) {
      constexpr int s = decltype(S)::v;
      if constexpr (j >= 1 && j + 2 < 8) {
        if (s == 0) gw(j + 2, 2);
        if (s == 1 && !TA) gw(j + 2, 3);
      }
      if (j == 2 && s == 0) {
        float c[8];
#pragma unroll
        for (int e = 0; e < 4; ++e) { c[2 * e] = blo(jc0[e]) + blo(jc1[e]); c[2 * e + 1] = bhi(jc0[e]) + bhi(jc1[e]); }
        float d[4], g[2];
#pragma unroll
        for (int e = 0; e < 4; ++e) d[e] = sw32_add(c[e], c[4 + e]);
#pragma unroll
        for (int e = 0; e < 2; ++e) { g[e] = sw16_add(d[e], d[2 + e]); g[e] = dpp_add<0x128>(g[e]); g[e] = dpp_add<0x124>(g[e]); }
        if ((l & 12) == 0) {
          const int col = (l & 3) * 8 + hh * 4 + blk * 2;
          LAS float* d0 = (LAS float*)(size_t)(lds0 + L_CSX + (col * 8 + w) * 4);
          d0[0] = g[0]; d0[8] = g[1];
        }
      }
      if (j == LDGJ && s == 0) { ldg(); if (!TA) ldb(); }
      awfill(JJ, IC<0>{}, S);
    });
    step(f1, f0, IC<1>{}, IC<2>{}, IC<sl>{}, IC<1>{}, [&](auto S) {
      constexpr int s = decltype(S)::v;
      if constexpr (j >= 1 && j + 3 < 8) { if (s == 0) gx(j + 3, 0); }
      if (s < tc_cnt(j)) touch(tc_base(j) + s);
    });
    step(f0, f1, IC<2>{}, IC<3>{}, IC<sl>{}, IC<1>{}, [&](auto S) {
      constexpr int s = decltype(S)::v;
      if constexpr (j == 7 && s == 0) {
        WAIT_V(ops_from(LDGJ, 0, 7, 1) - G - (LDGJ >= 1 && LDGJ + 2 < 8 ? 1 : 0));
        TIE(gst);
#pragma unroll
        for (int i = 0; i < NT; ++i) { TIE(gcs[i]); if (!TA) TIE(gb[i]); }
        for (int spin = 0; spin < SPIN_CAP; ++spin) {
          bool bad = gst[1] != tag || gst[3] != tag;
#pragma unroll
          for (int i = 0; i < NT; ++i) bad = bad || gcs[i][1] != tag;
          if (!any_set(bad)) break;
          __builtin_amdgcn_s_sleep(4);
          ldg();
          WAIT_V(0);
          TIE(gst);
#pragma unroll
          for (int i = 0; i < NT; ++i) TIE(gcs[i]);
        }
      }
      if constexpr (j >= 1 && j + 3 < 8) { if (s == 0) gx(j + 3, 1); }
      awfill(JJ, IC<1>{}, S);
    });
    if (j < 7) {
      WAIT_L0();
      constexpr int N = j == 0 ? NP + 2 + JP + ops_from(0, 0, 0, 2) : j == 1 ? ops_from(0, 0, 1, 2) : ops_from(j - 1, 1, j, 2);
      static_assert(N <= 63, "vmcnt range");
      WAIT_V(N);
      BAR();
    }
    step(f1, f0, IC<3>{}, IC<0>{}, IC<nsl>{}, IC<(j < 7)>{}, [&](auto S) {
      constexpr int s = decltype(S)::v;
      if constexpr (j + 3 < 8) {
        if (s == 0) gw(j + 3, 0);
        if (s == 1) gw(j + 3, 1);
      }
      if (TA && j == 0 && s == 2 && w == 0) {
        glds16((const char*)wphi + role * 2048, l * 16, lds0 + L_WPHI); glds16((const char*)wphi + role * 2048 + 1024, l * 16, lds0 + L_WPHI + 1024);
      }
      if (j == 1 && s == 1) {
        TIE(jx); TIE(jc0); TIE(jc1); TIE(jt);
        float v[8];
#pragma unroll
        for (int e = 0; e < 4; ++e) { v[2 * e] = blo(jx[e]); v[2 * e + 1] = bhi(jx[e]); }
        const float sm = ((v[0] + v[1]) + (v[2] + v[3])) + ((v[4] + v[5]) + (v[6] + v[7]));
        const float mean = wave_sum(sm) * (1.f / 512.f);
        float sq = 0.f;
#pragma unroll
        for (int e = 0; e < 8; ++e) { const float d = v[e] - mean; sq = __builtin_fmaf(d, d, sq); }
        const float rs = __builtin_amdgcn_rsqf(wave_sum(sq) * (1.f / 512.f) + 1e-5f);
        st4_m(ws + WS_ST, stok * 16, (u32x4){asu(mean), tag, asu(rs), tag}, 1ull);
      }
      if (j == 2 && s == 2) {
        if (w == 0 && panel < NCJ) {
          const f32x4 a = *(LAS const f32x4*)(size_t)(lds0 + L_CSX + t * 32), c = *(LAS const f32x4*)(size_t)(lds0 + L_CSX + t * 32 + 16);
          const float cs = ((a[0] + a[1]) + (a[2] + a[3])) + ((c[0] + c[1]) + (c[2] + c[3]));
          st2_m(ws + WS_CS, ((TA ? 0 : 1536) + ccol + t) * 16 + (cp & 1) * 8, (u32x2){asu(cs), tag}, 0xffffffffull);
        }
      }
      if constexpr (j == 7 && s == 0) {
        rstd = asf(gst[2]);
        float ca[4];
        float bop;
        if (TA) {
          bop = -asf(gst[0]);
#pragma unroll
          for (int i = 0; i < 3; ++i) ca[i] = asf(gcs[i][0]);
        } else {
          bop = hh ? __builtin_amdgcn_rcpf(rstd) : -asf(gst[0]);
#pragma unroll
          for (int i = 0; i < 4; ++i) { const float cs = sw32_add(asf(gcs[i][0]), asf(gcs[i][0])); ca[i] = hh ? asf(gb[i] << 16) : cs; }
          if (t >= 16) ca[3] = 0.f;
        }
        acc[1] = __builtin_amdgcn_mfma_f32_32x32x2f32(ca[1], bop, acc[1], 0, 0, 0);
        acc[2] = __builtin_amdgcn_mfma_f32_32x32x2f32(ca[2], bop, acc[2], 0, 0, 0);
        acc[0] = __builtin_amdgcn_mfma_f32_32x32x2f32(ca[0], bop, acc[0], 0, 0, 0);
        if (!TA) acc[3] = __builtin_amdgcn_mfma_f32_32x32x2f32(ca[3], bop, acc[3], 0, 0, 0);
      }
    });
    TS(3 + j);
  });
  constexpr int NW0 = aw_base(TA, 8);
  constexpr int NWL = NW0 + (TA ? ae_base(5) : 0);
  auto aep = [&](auto P) { constexpr int p = decltype(P)::v; if constexpr (ae_cnt(p) > 0) { SCHED(); sfor<NW0 + ae_base(p), NW0 + ae_base(p + 1)>(ldau); SCHED(); } };
  constexpr int NWM = TA ? NWL + APRE : NWL + NPRE < 28 ? NWL + NPRE : 28;
  u32x4 PQ[2][2];
  if (!TA) {
    *(LAS f32x4*)(size_t)(lds0 + L_T6 + w * 2048 + lo16) = (f32x4){acc[3][8], acc[3][9], acc[3][10], acc[3][11]};
    *(LAS f32x4*)(size_t)(lds0 + L_T6 + w * 2048 + 1024 + lo16) = (f32x4){acc[3][12], acc[3][13], acc[3][14], acc[3][15]};
    const f32x2 r2 = {rstd, rstd};
    const char* hfp = ws + WS_HF + (size_t)(tok32 * 112 + role * 14) * 1024;
    auto hpk = [&](float a, float c) { f32x2 u = (f32x2){a, c} * r2; u[0] = fmaxf(u[0], 0.f); u[1] = fmaxf(u[1], 0.f); return pk2v(u * u); };
    sfor<0, 3>([&](auto J) {
      constexpr int j = decltype(J)::v;
      u32x4 o0, o1;
#pragma unroll
      for (int e = 0; e < 4; ++e) { o0[e] = hpk(acc[j][2 * e], acc[j][2 * e + 1]); o1[e] = hpk(acc[j][8 + 2 * e], acc[j][9 + 2 * e]); }
      const char* d = hfp + (size_t)(6 * hf + 2 * j) * 1024;
      st4<0>(d, lo16, o0); st4<1024>(d, lo16, o1);
    });
    TS(22);
    WAIT_L0();
    BAR();
    TS(23); TS(24);
    {
      const f32x4 pa = *(LAS const f32x4*)(size_t)(lds0 + L_T6 + (w ^ 4) * 2048 + lo16);
      const f32x4 pb = *(LAS const f32x4*)(size_t)(lds0 + L_T6 + (w ^ 4) * 2048 + 1024 + lo16);
      u32x4 o;
      o[0] = hpk(acc[3][0] + pa[0], acc[3][1] + pa[1]); o[1] = hpk(acc[3][2] + pa[2], acc[3][3] + pa[3]);
      o[2] = hpk(acc[3][4] + pb[0], acc[3][5] + pb[1]); o[3] = hpk(acc[3][6] + pb[2], acc[3][7] + pb[3]);
      st4<0>(hfp + (size_t)(12 + hf) * 1024, lo16, o);
    }
    TS(11);
    BAR();
  } else {
    aep(IC<0>{});
    const unsigned sgn = ((t >> 2) & 1) ? 0x80008000u : 0u;
    bf16x8 aphi[2];
#pragma unroll
    for (int e = 0; e < 2; ++e) {
      const unsigned a = lds0 + L_WPHI + (2 * hf + e) * 512 + (4 * hh + q) * 32 + (8 * blk + 4 * (p >> 1)) * 2;
      u32x4 u = __builtin_bit_cast(u32x4, tr_frag(a, a + 8 * 32));
      u[0] ^= sgn; u[1] ^= sgn; u[2] ^= sgn; u[3] ^= sgn;
      aphi[e] = __builtin_bit_cast(bf16x8, u);
    }
    const float rl2 = rstd * 1.4426950408889634f;
    const int tok = 32 * tg + t, xs = (tok >> 1) & 3;
    sfor<0, 2>([&](auto EE) {
      constexpr int e = decltype(EE)::v;
      const bf16x8 kf = pack8n(acc[1], 8 * e);
      const f32x16 pp = mfma32(aphi[e], kf, (f32x16){});
      u32x4 g0, g1; softmax16(pp, rl2, g0, g1);
      const unsigned r = lds0 + L_PK + (2 * hf + e) * 8192 + tok * 64;
      *(LAS u32x4*)(size_t)(r + (((2 * hh) ^ xs) << 4)) = g0;
      *(LAS u32x4*)(size_t)(r + (((2 * hh + 1) ^ xs) << 4)) = g1;
      aep(IC<1 + e>{});
    });
    {
      const unsigned r = lds0 + L_VI + hf * 8192 + tok * 64 + 8 * hh;
#pragma unroll
      for (int g = 0; g < 4; ++g) {
        u32x2 o;
        const f32x2 r2 = {rstd, rstd};
        o[0] = pk2v((f32x2){acc[2][4 * g], acc[2][4 * g + 1]} * r2);
        o[1] = pk2v((f32x2){acc[2][4 * g + 2], acc[2][4 * g + 3]} * r2);
        *(LAS u32x2*)(size_t)(r + ((g ^ xs) << 4)) = o;
      }
    }
    aep(IC<3>{});
    TS(22);
    WAIT_L0();
    BAR();
    TS(23);
    aep(IC<4>{});
    auto phiq = [&]() {
#pragma unroll
      for (int e = 0; e < 2; ++e) {
        const bf16x8 qf = pack8n(acc[0], 8 * e);
        const f32x16 pp = mfma32(aphi[e], qf, (f32x16){});
        softmax16(pp, rl2, PQ[e][0], PQ[e][1]);
      }
    };
    if (hf == 1) {
      f32x16 kv = (f32x16){};
      {
        const int r0 = 4 * hh + q, xr = (r0 >> 1) & 3;
        const unsigned a0 = lds0 + L_PK + tg * 8192 + r0 * 64 + (((2 * blk + (p >> 1)) ^ xr) << 4) + 8 * (p & 1);
        const unsigned b0 = blk ? (lds0 + L_ONES + r0 * 64) : (lds0 + L_VI + (tg >> 1) * 8192 + r0 * 64 + (((2 * (tg & 1) + (p >> 1)) ^ xr) << 4) + 8 * (p & 1));
        bf16x8 fa[8], fb[8];
#pragma unroll
        for (int u = 0; u < 8; ++u) { fa[u] = tr_frag(a0 + u * 1024, a0 + u * 1024 + 512); fb[u] = tr_frag(b0 + u * 1024, b0 + u * 1024 + 512); }
        SCHED();
#pragma unroll
        for (int u = 0; u < 8; ++u) kv = mfma32(fa[u], fb[u], kv);
      }
      const char* kp = ws + WS_KVP + (size_t)((role * 4 + tg) * 8 * 16 + panel) * 1024;
      sfor<0, 8>([&](auto J) {
        constexpr int j = decltype(J)::v;
        st4_m(kp + (size_t)j * 16384, lo16, (u32x4){asu(kv[2 * j]), tag, asu(kv[2 * j + 1]), tag}, 0x0001ffff0001ffffull);
      });
      TS(24);
      WAIT_L0();
      BAR();
      phiq();
    } else {
      phiq();
      TS(24);
      BAR();
    }
    TS(11);
  }
  const unsigned R = lds0 + w * P2_WS;
  const char* wob = (const char*)wo + (size_t)(8 * w) * 8192 + ct * 128;
  auto gwo = [&]() {
#pragma unroll
    for (int u = 0; u < 8; ++u) glds16(wob + (size_t)u * 8192, uoff, R + u * 1024);
  };
  sfor<NWL, NWM>(ldau);
  if (!TA) {
    WAIT_V(NWM - NWL);
    st1_m(ws + WS_FH + (128 + panel * 8 + role) * 128, (tg * 2 + hf) * 4, tag, 1ull);
  }
  TS(12);
  if ((AJOB ? TA : !TA) && w < (JDUP ? 4 : 2)) {
    const int jid = (w < 2 ? panel * 8 + role : (panel ^ 8) * 8 + (role ^ 1)) * 2 + (w & 1), hd = jid >> 3, jj = jid & 7;
    const char* src = ws + WS_KVP + (size_t)(hd * 8 + jj) * 16384;
    const unsigned vo = hh * 1024 + blk * 512 + (l & 15) * 16;
    const unsigned vz = (l & 15) * 1024 + blk * 512 + 256;
    u32x4 pv[8], pz;
    for (int spin = 0; spin < SPIN_CAP; ++spin) {
      sfor<0, 8>([&](auto I) { constexpr int i = decltype(I)::v; ald4c<(i & 1) * 2048>(pv[i], src + (i >> 1) * 4096, vo); });
      ald4c<0>(pz, src, vz);
      WAIT_V(0);
      bool bad = false;
#pragma unroll
      for (int i = 0; i < 8; ++i) { TIE(pv[i]); bad = bad || pv[i][1] != tag || pv[i][3] != tag; }
      TIE(pz); bad = bad || pz[1] != tag || pz[3] != tag;
      if (!any_set(bad)) break;
      __builtin_amdgcn_s_sleep(JSL);
    }
    float s0 = 0.f, s1 = 0.f;
#pragma unroll
    for (int i = 0; i < 8; ++i) { s0 += asf(pv[i][0]); s1 += asf(pv[i][2]); }
    s0 = sw32_add(s0, s0); s1 = sw32_add(s1, s1);
    float z0 = asf(pz[0]), z1 = asf(pz[2]);
    z0 = dpp_add<0x128>(z0); z0 = dpp_add<0x124>(z0); z0 = dpp_add<0x122>(z0); z0 = dpp_add<0x121>(z0);
    z1 = dpp_add<0x128>(z1); z1 = dpp_add<0x124>(z1); z1 = dpp_add<0x122>(z1); z1 = dpp_add<0x121>(z1);
    const unsigned val = hh ? pk2(z0, z1) : pk2(s0, s1);
    const unsigned c = hh ? 16 : (l & 15);
    st2_m(ws + WS_KVZ + (size_t)(hd * 4 + (jj >> 1)) * 544, c * 32 + (2 * blk + (jj & 1)) * 8, (u32x2){val, tag}, 0x00010001ffffffffull);
  }
  sfor<NWM, 28>(ldau);
  gwo();
  bf16x8 AW[14][2];
  const unsigned ub = R + (4 * hh + q) * 128 + blk * 32 + 8 * p, us = 64 * (q >> 1);
  const unsigned ua0 = ub + us, ua1 = ub + (64 ^ us);
  auto rdu = [&](int c, int o) { const unsigned a = (c ? ua1 : ua0) + o; return tr_frag(a, a + 1024); };
  sfor<0, 14>([&](auto N) {
    constexpr int n = decltype(N)::v;
    WAIT_V(35 - (2 * n + 1) < 63 ? 35 - (2 * n + 1) : 63);
    tie4(AU[2 * n]); tie4(AU[2 * n + 1]);
    if (n == 13) tie1(jw);
    constexpr int o = 8192 + (n & 3) * 2048;
    *(LAS u32x4*)(size_t)(R + o + lo16) = AU[2 * n]; *(LAS u32x4*)(size_t)(R + o + 1024 + lo16) = AU[2 * n + 1];
    AW[n][0] = rdu(0, o); AW[n][1] = rdu(1, o);
    SCHED();
  });
  TS(17);
  if (TA) {
    {
      const int hd = role * 4 + 2 * hf + (tg >> 1);
      const char* kzb = ws + WS_KVZ + (size_t)(hd * 4 + (tg & 1)) * 544;
      const unsigned kzo = (t < 16 ? t : 16) * 32 + hh * 1088;
      const bool kzl = t <= 16 || t == 20;
      u32x4 ka, kb;
      for (int spin = 0; spin < SPIN_CAP; ++spin) {
        aldkz<0>(ka, kb, kzb, kzo);
        WAIT_V(0);
        tie4(ka); tie4(kb);
        const bool bad = ka[1] != tag || ka[3] != tag || kb[1] != tag || kb[3] != tag;
        if (!any_set(bad && kzl)) break;
        __builtin_amdgcn_s_sleep(KSL);
      }
      *(LAS u32x4*)(size_t)(lds0 + L_ONES + w * 1024 + lo16) = (u32x4){ka[0], ka[2], kb[0], kb[2]};
    }
    WAIT_L0();
    BAR();
    TS(15);
    const char* op = ws + WS_PQ + (size_t)(tok32 * 32 + role * 4 + 2 * hf) * 1024;
#pragma unroll
    for (int e = 0; e < 2; ++e) {
      const u32x4 k0 = *(LAS const u32x4*)(size_t)(lds0 + L_ONES + (4 * hf + 2 * e) * 1024 + lo16);
      const u32x4 k1 = *(LAS const u32x4*)(size_t)(lds0 + L_ONES + (4 * hf + 2 * e + 1) * 1024 + lo16);
      f32x16 nm = mfma32u(k0, PQ[e][0], (f32x16){});
      nm = mfma32u(k1, PQ[e][1], nm);
      const float rd = __builtin_amdgcn_rcpf(nm[8] + 1e-6f);
      const u32x4 of = __builtin_bit_cast(u32x4, pack8(nm, 0, rd));
      if (e == 0) st4<0>(op, lo16, of); else st4<1024>(op, lo16, of);
    }
    TS(16);
  }
  {
    const unsigned fvo = (128 + (tt >> 1) * 8 + w) * 128 + ((tt & 1) * 4 + (l & 3)) * 4;
    for (int spin = 0; spin < SPIN_CAP; ++spin) {
      unsigned f;
      ald1c<0>(f, ws + WS_FH, fvo);
      WAIT_V(0);
      TIE(f);
      if (TA && spin == 0) st1_m(ws + WS_FH + (panel * 8 + role) * 128, (tg * 2 + hf) * 4, tag, 1ull);
      if (!any_set(f != tag)) break;
      __builtin_amdgcn_s_sleep(HSL);
    }
  }
  TS(13);
  {
  int l2;
  asm volatile("v_mbcnt_lo_u32_b32 %0, -1, 0\n\tv_mbcnt_hi_u32_b32 %0, -1, %0" : "=v"(l2));
  const int t = l2 & 31, hh = l2 >> 5, tid = w * 64 + l2;
  const unsigned lo16 = l2 * 16;
  const __amdgpu_buffer_rsrc_t rs = __builtin_amdgcn_make_buffer_rsrc((void*)ws, 0, (int)WS_END, 0x00020000);
  const unsigned hp = WS_HF + ((2 * tt) * 112 + 14 * w) * 1024;
  const unsigned op = WS_PQ + ((2 * tt) * 32 + 4 * w) * 1024;
  auto ldc = [&](unsigned so) { return __builtin_amdgcn_raw_buffer_load_b128(rs, lo16, so, 16); };
  u32x4 B0[18], B1[18];
  auto ldh = [&](int n) {
    if (n < 14) { B0[n] = ldc(hp + n * 1024); B1[n] = ldc(hp + (112 + n) * 1024); }
    else { B0[n] = ldc(op + (n - 14) * 1024); B1[n] = ldc(op + (32 + n - 14) * 1024); }
  };
#pragma unroll
  for (int n = 0; n < P2_D; ++n) ldh(n);
  SCHED();
  f32x16 ac[2][2];
#pragma unroll
  for (int i = 0; i < 2; ++i)
#pragma unroll
    for (int j = 0; j < 2; ++j) ac[i][j] = (f32x16){};
  gu32* fpq = (gu32*)(ws + WS_FH + ((tt >> 1) * 8 + w) * 128 + ((tt & 1) * 4 + (l2 & 3)) * 4);
  unsigned flq = 0;
  const int tokr = tid >> 3, n0 = (tid & 7) * 8;
  const size_t go = (size_t)(tt * 64 + tokr) * 512 + ct * 64 + n0;
  u32x4 xa, ba;
  bf16x8 wo0, wo1;
  constexpr auto req = [](int n) { const int r = P2_D + 2 * n; return r < 14 ? r : 14; };
#pragma unroll
  for (int n = 0; n < 18; ++n) {
    if (n == OFN) flq = __hip_atomic_load(fpq, RLX_AGENT);
    if (n == OFN + 3) {
      for (int spin = 0; spin < SPIN_CAP && any_set(flq != tag); ++spin) { __builtin_amdgcn_s_sleep(OSL); flq = __hip_atomic_load(fpq, RLX_AGENT); }
#pragma unroll
      for (int m = 14; m < 18; ++m) ldh(m);
    }
    if (n == 15) { xa = *(const u32x4*)(x + go); ba = *(const u32x4*)(b2 + ct * 64 + n0); }
    bf16x8 w0, w1;
    if (n < 14) { w0 = AW[n][0]; w1 = AW[n][1]; } else { w0 = wo0; w1 = wo1; }
    if (n >= 13 && n < 17) { wo0 = rdu(0, (n - 13) * 2048); wo1 = rdu(1, (n - 13) * 2048); }
    SCHED();
    ac[0][0] = mfma32(w0, __builtin_bit_cast(bf16x8, B0[n]), ac[0][0]);
    ac[0][1] = mfma32(w0, __builtin_bit_cast(bf16x8, B1[n]), ac[0][1]);
    ac[1][0] = mfma32(w1, __builtin_bit_cast(bf16x8, B0[n]), ac[1][0]);
    ac[1][1] = mfma32(w1, __builtin_bit_cast(bf16x8, B1[n]), ac[1][1]);
    SCHED();
    for (int m = req(n); m < req(n + 1); ++m) ldh(m);
    SCHED();
    if (n == 0) TS(14);
    if (n == 13) TS(18);
  }
  TS(19);
#pragma unroll
  for (int j = 0; j < 2; ++j)
#pragma unroll
    for (int c = 0; c < 2; ++c)
#pragma unroll
      for (int g = 0; g < 4; ++g)
        *(LAS f32x4*)(size_t)(R + (32 * j + t) * P2_RS + (32 * c + 8 * g + 4 * hh) * 4) = (f32x4){ac[c][j][4 * g], ac[c][j][4 * g + 1], ac[c][j][4 * g + 2], ac[c][j][4 * g + 3]};
  WAIT_L0();
  BAR();
  {
    const unsigned ra = lds0 + tokr * P2_RS + n0 * 4;
    f32x4 s0 = *(LAS const f32x4*)(size_t)ra, s1 = *(LAS const f32x4*)(size_t)(ra + 16);
#pragma unroll
    for (int ww = 1; ww < 8; ++ww) {
      s0 += *(LAS const f32x4*)(size_t)(ra + ww * P2_WS);
      s1 += *(LAS const f32x4*)(size_t)(ra + ww * P2_WS + 16);
    }
    u32x4 o;
    o[0] = pk2(s0[0] + blo(xa[0]) + blo(ba[0]), s0[1] + bhi(xa[0]) + bhi(ba[0]));
    o[1] = pk2(s0[2] + blo(xa[1]) + blo(ba[1]), s0[3] + bhi(xa[1]) + bhi(ba[1]));
    o[2] = pk2(s1[0] + blo(xa[2]) + blo(ba[2]), s1[1] + bhi(xa[2]) + bhi(ba[2]));
    o[3] = pk2(s1[2] + blo(xa[3]) + blo(ba[3]), s1[3] + bhi(xa[3]) + bhi(ba[3]));
    asm volatile("global_store_dwordx4 %0, %1, %2 sc1 nt" ::"v"((unsigned)(go * 2)), "v"(o), "s"(out) : "memory");
  }
  }
  if (b == 0 && w == 0) st1_m(ws + WS_E, 0, tag, 1ull);
  TS(20);
  TS(21); TS(25);
  TS_OUT();
}

__global__ __launch_bounds__(512, 2) void k_fused(const u16* __restrict__ x, const u16* __restrict__ wqkv, const u16* __restrict__ wphi,
                                                  const u16* __restrict__ wo, const u16* __restrict__ w1, const u16* __restrict__ b1,
                                                  const u16* __restrict__ w2, const u16* __restrict__ b2, char* __restrict__ ws, u16* __restrict__ out) {
  extern __shared__ __attribute__((aligned(16))) char smem[];
  const unsigned lds0 = (unsigned)(size_t)(LAS char*)smem;
  const int b = blockIdx.x;
  if ((b >> 6) < 2) body<true>(lds0, b, x, wqkv, wphi, wo, w1, b1, w2, b2, ws, out);
  else body<false>(lds0, b, x, wqkv, wphi, wo, w1, b1, w2, b2, ws, out);
}

extern "C" void kernel_launch(void* const* d_in, const int* in_sizes, int n_in, void* d_out, int out_size, void* d_ws, size_t ws_size, hipStream_t stream) {
  const u16* x = (const u16*)d_in[0];
  const u16* wqkv = (const u16*)d_in[1];
  const u16* wphi = (const u16*)d_in[2];
  const u16* wo = (const u16*)d_in[3];
  const u16* w1 = (const u16*)d_in[5];
  const u16* b1 = (const u16*)d_in[6];
  const u16* w2 = (const u16*)d_in[7];
  const u16* b2 = (const u16*)d_in[8];
  hipFuncSetAttribute((const void*)k_fused, hipFuncAttributeMaxDynamicSharedMemorySize, LDS_BYTES);
  k_fused<<<256, 512, LDS_BYTES, stream>>>(x, wqkv, wphi, wo, w1, b1, w2, b2, (char*)d_ws, (u16*)d_out);
}
```
